```python
import torch

def _softmax(x, axis=-1):
    return torch.softmax(x, dim=axis)
def _gelu(x):
    return torch.nn.functional.gelu(x, approximate="tanh")
def _silu(x):
    return torch.nn.functional.silu(x)
def _sigmoid(x):
    return torch.sigmoid(x)
def _layernorm(x, axis=-1, eps=1e-5):
    m = x.mean(dim=axis, keepdim=True)
    v = x.var(dim=axis, keepdim=True, unbiased=False)
    return (x - m) / torch.sqrt(v + eps)
def _scatter_add(tgt, idx, src, axis=0):
    return tgt.clone().index_add_(axis, idx, src)
def _segment_sum(src, seg, n):
    out = torch.zeros((n,) + src.shape[1:], dtype=src.dtype)
    return out.index_add_(0, seg, src)
def _pad(x, pw):
    flat = tuple(v for pair in reversed(pw) for v in pair)
    return torch.nn.functional.pad(x, flat)

kernel_name = 'sequential_preLN__spectral_fnet__moe_arch_mod__prompt'

def setup_inputs():

    _w = (lambda a: (lambda t: t.to(torch.bfloat16) if t.is_floating_point() else t)(torch.as_tensor(a)))
    def _A_setup():
        B, S, D = 1, 2048, 512
        ns = torch.arange(S)[:, None] * torch.arange(S)[None, :]
        nd = torch.arange(D)[:, None] * torch.arange(D)[None, :]
        Ws_re = torch.cos(-2.0 * torch.pi * ns / S).to(float)
        Ws_im = torch.sin(-2.0 * torch.pi * ns / S).to(float)
        Wd_re = torch.cos(-2.0 * torch.pi * nd / D).to(float)
        Wd_im = torch.sin(-2.0 * torch.pi * nd / D).to(float)
        return {
            "x": _w((lambda *s: torch.randn(*s, dtype=torch.bfloat16) * 0.05)(B, S, D)),
            "dft_seq_re": _w(Ws_re),
            "dft_seq_im": _w(Ws_im),
            "dft_hid_re": _w(Wd_re),
            "dft_hid_im": _w(Wd_im),
        }
    _A_in = _A_setup()

    def _B_setup():
        B, S, D, F, C = 1, 2048, 512, 1792, 1
        return {
            "x": _w((lambda *s: torch.randn(*s, dtype=torch.bfloat16) * 0.05)(B, S, D)),
            "w_r": _w((lambda *s: torch.randn(*s, dtype=torch.bfloat16) * 1.6)(D)),
            "w_gate": _w((lambda *s: torch.randn(*s, dtype=torch.bfloat16) * 0.4)(D, F)),
            "w_up": _w((lambda *s: torch.randn(*s, dtype=torch.bfloat16) * 0.4)(D, F)),
            "w_down": _w((lambda *s: torch.randn(*s, dtype=torch.bfloat16) * 0.4)(F, D)),
        }
    _B_in = _B_setup()

    return {
        "A_x": _A_in["x"],
        "A_dft_seq_re": _A_in["dft_seq_re"],
        "A_dft_seq_im": _A_in["dft_seq_im"],
        "A_dft_hid_re": _A_in["dft_hid_re"],
        "A_dft_hid_im": _A_in["dft_hid_im"],
        "B_x": _B_in["x"],
        "B_w_r": _B_in["w_r"],
        "B_w_gate": _B_in["w_gate"],
        "B_w_up": _B_in["w_up"],
        "B_w_down": _B_in["w_down"],
    }

def reference(A_x, A_dft_seq_re, A_dft_seq_im, A_dft_hid_re, A_dft_hid_im, B_x, B_w_r, B_w_gate, B_w_up, B_w_down):
    def _A_ref(x, dft_seq_re, dft_seq_im, dft_hid_re, dft_hid_im):
        a = torch.einsum("bsd,ed->bse", x, dft_hid_re)
        b = torch.einsum("bsd,ed->bse", x, dft_hid_im)
        y = torch.einsum("ts,bse->bte", dft_seq_re, a) - torch.einsum("ts,bse->bte", dft_seq_im, b)
        return y

    def _B_ref(x, w_r, w_gate, w_up, w_down):
        B, S, D = x.shape
        C = 1
        r = x @ w_r
        idx = torch.argsort(-r, axis=-1)[:, :C]
        idx_e = (lambda x, n, axis: torch.repeat_interleave(x, n, dim=axis))(idx.reshape(B, C, 1), D, axis=2)
        x_sel = (lambda x, i, axis: torch.take_along_dim(x, i, dim=axis))(x, idx_e, axis=1)
        g = _silu(x_sel @ w_gate)
        u = x_sel @ w_up
        y = (g * u) @ w_down
        r_sel = _sigmoid((lambda x, i, axis: torch.take_along_dim(x, i, dim=axis))(r, idx, axis=1))
        y = y * r_sel.reshape(B, C, 1)
        b_off = (torch.arange(B) * S).reshape(B, 1)
        flat_idx = (idx + b_off).reshape(B * C)
        zero = (x * 0.0).reshape(B * S, D)
        upd = _scatter_add(zero, flat_idx, y.reshape(B * C, D), axis=0)
        return x + upd.reshape(B, S, D)

    x = A_x
    h = x + _A_ref(_layernorm(x, axis=-1), A_dft_seq_re, A_dft_seq_im, A_dft_hid_re, A_dft_hid_im)
    h2 = h + _B_ref(_layernorm(h, axis=-1), B_w_r, B_w_gate, B_w_up, B_w_down)
    return h2
```

```cpp
#include <hip/hip_runtime.h>
#include <stdint.h>

typedef unsigned short u16;
typedef short bf16x8 __attribute__((ext_vector_type(8)));
typedef __bf16 bf16x2_t __attribute__((ext_vector_type(2)));
typedef float f32x16 __attribute__((ext_vector_type(16)));
typedef float f32x4 __attribute__((ext_vector_type(4)));
typedef float f32x2 __attribute__((ext_vector_type(2)));
typedef unsigned u32x4 __attribute__((ext_vector_type(4)));
typedef unsigned u32x2 __attribute__((ext_vector_type(2)));
#define DI __device__ __forceinline__
#define LAS __attribute__((address_space(3)))
#define MFMA32(a, b, c) __builtin_amdgcn_mfma_f32_32x32x16_bf16((a), (b), (c), 0, 0, 0)

constexpr int S_ = 2048, D_ = 512, F_ = 1792;

DI float bf_lo(unsigned w) { return __uint_as_float(w << 16); }
DI float bf_hi(unsigned w) { return __uint_as_float(w & 0xffff0000u); }
DI unsigned pk_bf16(float lo, float hi) {
  f32x2 v = {lo, hi};
  bf16x2_t b = __builtin_convertvector(v, bf16x2_t);
  return __builtin_bit_cast(unsigned, b);
}
DI float rbf(float x) {
  return bf_lo(pk_bf16(x, 0.f) & 0xffffu);
}
DI float dot2(unsigned a, unsigned b, float acc) {
  return __builtin_amdgcn_fdot2_f32_bf16(__builtin_bit_cast(bf16x2_t, a), __builtin_bit_cast(bf16x2_t, b), acc, false);
}
DI void glds16(const void* g, LAS void* l) {
  __builtin_amdgcn_global_load_lds((const __attribute__((address_space(1))) unsigned*)g, (LAS unsigned*)l, 16, 0, 0);
}

constexpr size_t WS_AB = 0;
constexpr size_t WS_PART = WS_AB + (size_t)4096 * 512 * 2;
constexpr size_t WS_CAND = WS_PART + (size_t)4 * 2048 * 512 * 4;
constexpr size_t WS_CROW = WS_CAND + 4096;
constexpr size_t WS_ACT = WS_CROW + 256 * 2048;
constexpr size_t WS_END = WS_ACT + 8192;

__global__ __launch_bounds__(256, 1) void k1_ln_gemm(const u16* __restrict__ x, const u16* __restrict__ dr,
                                                     const u16* __restrict__ di, unsigned char* __restrict__ ws) {
  __shared__ __attribute__((aligned(16))) unsigned char lds[65536 + 1024 + 1024];
  LAS unsigned char* L = (LAS unsigned char*)lds;
  const int tid = threadIdx.x, w = __builtin_amdgcn_readfirstlane(tid >> 6), l = tid & 63, r = l & 31, h = l >> 5;
  const int b = blockIdx.x, xcd = b & 7, j = b >> 3;
  const int sb = (xcd >> 1) * 4 + (j & 3), eb = (xcd & 1) * 8 + (j >> 2);
  const int isB = eb >> 3;
  const u16* dm = (isB ? di : dr) + (size_t)(eb & 7) * 64 * D_;

  u32x4 xf[32];
  const u16* xrow = x + (size_t)(sb * 128 + w * 32 + r) * D_ + 8 * h;
#pragma unroll
  for (int ks = 0; ks < 32; ++ks) xf[ks] = *(const u32x4*)(xrow + 16 * ks);
#pragma unroll
  for (int i = 0; i < 16; ++i) {
    const int n = w * 16 + i;
    glds16(dm + (size_t)n * D_ + ((l ^ (n & 15)) * 8), L + n * 1024 + l * 16);
  }
  float s = 0.f;
#pragma unroll
  for (int ks = 0; ks < 32; ++ks)
#pragma unroll
    for (int q = 0; q < 4; ++q) s = dot2(xf[ks][q], 0x3f803f80u, s);
  s += __shfl_xor(s, 32);
  const float mean = s * (1.0f / D_);
  float v0 = 0.f, v1 = 0.f;
#pragma unroll
  for (int ks = 0; ks < 32; ++ks)
#pragma unroll
    for (int q = 0; q < 4; ++q) {
      const float d0 = bf_lo(xf[ks][q]) - mean, d1 = bf_hi(xf[ks][q]) - mean;
      v0 = fmaf(d0, d0, v0);
      v1 = fmaf(d1, d1, v1);
    }
  float var = v0 + v1;
  var += __shfl_xor(var, 32);
  var *= (1.0f / D_);
  {
    const float m_b = rbf(mean);
    const float sd = rbf(sqrtf(rbf(rbf(var) + 1e-5f)));
    float rs = 1.0f / sd;
    if (isB) rs = -rs;
    LAS f32x2* st = (LAS f32x2*)(L + 65536);
    if (h == 0) st[w * 32 + r] = (f32x2){m_b, rs};
  }
  asm volatile("s_waitcnt vmcnt(0)" ::: "memory");
  __syncthreads();

  f32x16 acc0 = {}, acc1 = {};
  float sd0 = 0.f, sd1 = 0.f;
  const int sw = r & 15;
#pragma unroll
  for (int ks = 0; ks < 32; ++ks) {
    const int pos = ((2 * ks + h) ^ sw) * 16;
    const u32x4 b0 = *(const LAS u32x4*)(L + r * 1024 + pos);
    const u32x4 b1 = *(const LAS u32x4*)(L + (32 + r) * 1024 + pos);
    acc0 = MFMA32(__builtin_bit_cast(bf16x8, xf[ks]), __builtin_bit_cast(bf16x8, b0), acc0);
    acc1 = MFMA32(__builtin_bit_cast(bf16x8, xf[ks]), __builtin_bit_cast(bf16x8, b1), acc1);
    if ((ks >> 3) == w) {
#pragma unroll
      for (int q = 0; q < 4; ++q) { sd0 = dot2(b0[q], 0x3f803f80u, sd0); sd1 = dot2(b1[q], 0x3f803f80u, sd1); }
    }
  }
  sd0 += __shfl_xor(sd0, 32);
  sd1 += __shfl_xor(sd1, 32);
  LAS float* sdl = (LAS float*)(L + 65536 + 1024);
  if (h == 0) { sdl[w * 64 + r] = sd0; sdl[w * 64 + 32 + r] = sd1; }
  __syncthreads();
  const float cs0 = (sdl[r] + sdl[64 + r]) + (sdl[128 + r] + sdl[192 + r]);
  const float cs1 = (sdl[32 + r] + sdl[96 + r]) + (sdl[160 + r] + sdl[224 + r]);
  const LAS f32x4* st4 = (const LAS f32x4*)(L + 65536 + w * 256);
  unsigned p0[8], p1[8];
#pragma unroll
  for (int g = 0; g < 4; ++g) {
    const f32x4 sa = st4[(8 * g + 4 * h) / 2], sbv = st4[(8 * g + 4 * h) / 2 + 1];
    const float m0 = sa[0], r0 = sa[1], m1 = sa[2], r1 = sa[3], m2 = sbv[0], r2 = sbv[1], m3 = sbv[2], r3 = sbv[3];
    p0[2 * g] = pk_bf16((acc0[4 * g] - m0 * cs0) * r0, (acc0[4 * g + 1] - m1 * cs0) * r1);
    p0[2 * g + 1] = pk_bf16((acc0[4 * g + 2] - m2 * cs0) * r2, (acc0[4 * g + 3] - m3 * cs0) * r3);
    p1[2 * g] = pk_bf16((acc1[4 * g] - m0 * cs1) * r0, (acc1[4 * g + 1] - m1 * cs1) * r1);
    p1[2 * g + 1] = pk_bf16((acc1[4 * g + 2] - m2 * cs1) * r2, (acc1[4 * g + 3] - m3 * cs1) * r3);
  }
  const int kk0 = isB * 2048 + sb * 128 + w * 32;
  const int kb = kk0 >> 6, kst = (kk0 >> 4) & 3;
  const int n0 = (eb & 7) * 64, nb = n0 >> 7, nt0 = (n0 >> 5) & 3;
  unsigned char* ab = ws + WS_AB + ((size_t)(nb * 64 + kb) * 16) * 1024 + l * 16;
#pragma unroll
  for (int s2 = 0; s2 < 2; ++s2) {
    {
      auto t0 = __builtin_amdgcn_permlane32_swap(p0[4 * s2], p0[4 * s2 + 2], false, false);
      auto t1 = __builtin_amdgcn_permlane32_swap(p0[4 * s2 + 1], p0[4 * s2 + 3], false, false);
      u32x4 o = {t0[0], t1[0], t0[1], t1[1]};
      *(u32x4*)(ab + ((kst + s2) * 4 + nt0) * 1024) = o;
    }
    {
      auto t0 = __builtin_amdgcn_permlane32_swap(p1[4 * s2], p1[4 * s2 + 2], false, false);
      auto t1 = __builtin_amdgcn_permlane32_swap(p1[4 * s2 + 1], p1[4 * s2 + 3], false, false);
      u32x4 o = {t0[0], t1[0], t0[1], t1[1]};
      *(u32x4*)(ab + ((kst + s2) * 4 + nt0 + 1) * 1024) = o;
    }
  }
}

constexpr int K2_NST = 4;
__global__ __launch_bounds__(256, 1) void k2_gemm(const u16* __restrict__ cr, const u16* __restrict__ ci,
                                                  unsigned char* __restrict__ ws) {
  extern __shared__ __attribute__((aligned(16))) unsigned char smem[];
  LAS unsigned char* L = (LAS unsigned char*)smem;
  const int tid = threadIdx.x, w = __builtin_amdgcn_readfirstlane(tid >> 6), l = tid & 63, r = l & 31, h = l >> 5;
  const int b = blockIdx.x, xcd = b & 7, j = b >> 3;
  const int ks = xcd >> 1, mb = (xcd & 1) * 8 + (j >> 2), nb = j & 3;
  const int wm = w >> 1, wn = w & 1;
  const u16* A = ((ks >> 1) ? ci : cr) + (size_t)mb * 128 * S_ + (ks & 1) * 1024;
  const unsigned char* B = ws + WS_AB + (size_t)(nb * 64 + ks * 16) * 16384;
  const u16* asrc[4];
#pragma unroll
  for (int i = 0; i < 4; ++i) {
    const int row = 8 * (4 * w + i) + (l >> 3);
    const int c = (l & 7) ^ ((row >> 1) & 7);
    asrc[i] = A + (size_t)row * S_ + c * 8;
  }
  const unsigned char* bsrc = B + w * 4096 + l * 16;
  auto stage = [&](int t, int buf) {
#pragma unroll
    for (int i = 0; i < 4; ++i) glds16(asrc[i] + t * 64, L + buf * 32768 + (4 * w + i) * 1024 + l * 16);
#pragma unroll
    for (int i = 0; i < 4; ++i) glds16(bsrc + (size_t)t * 16384 + i * 1024, L + buf * 32768 + 16384 + (4 * w + i) * 1024 + l * 16);
  };
  f32x16 acc[2][2] = {};
  int aoff[4];
  const int sw = (r >> 1) & 7;
#pragma unroll
  for (int kk = 0; kk < 4; ++kk) aoff[kk] = (wm * 64 + r) * 128 + (((2 * kk + h) ^ sw) * 16);
  const int boff = 16384 + wn * 2048 + l * 16;

  stage(0, 0); stage(1, 1); stage(2, 2);
#pragma unroll
  for (int t = 0; t < 16; ++t) {
    if (t < 14) asm volatile("s_waitcnt vmcnt(16)" ::: "memory");
    else if (t == 14) asm volatile("s_waitcnt vmcnt(8)" ::: "memory");
    else asm volatile("s_waitcnt vmcnt(0)" ::: "memory");
    asm volatile("s_waitcnt lgkmcnt(0)" ::: "memory");
    __builtin_amdgcn_s_barrier();
    asm volatile("" ::: "memory");
    if (t + 3 < 16) stage(t + 3, (t + 3) & 3);
    const int bo = (t & 3) * 32768;
#pragma unroll
    for (int kk = 0; kk < 4; ++kk) {
      const bf16x8 a0 = *(const LAS bf16x8*)(L + bo + aoff[kk]);
      const bf16x8 a1 = *(const LAS bf16x8*)(L + bo + aoff[kk] + 4096);
      const bf16x8 b0 = *(const LAS bf16x8*)(L + bo + boff + kk * 4096);
      const bf16x8 b1 = *(const LAS bf16x8*)(L + bo + boff + kk * 4096 + 1024);
      acc[0][0] = MFMA32(b0, a0, acc[0][0]);
      acc[0][1] = MFMA32(b1, a0, acc[0][1]);
      acc[1][0] = MFMA32(b0, a1, acc[1][0]);
      acc[1][1] = MFMA32(b1, a1, acc[1][1]);
    }
  }
  float* P = (float*)(ws + WS_PART) + ((((size_t)ks * 16 + mb) * 4 + nb) * 4 + w) * 4096 + l * 4;
#pragma unroll
  for (int mt = 0; mt < 2; ++mt)
#pragma unroll
    for (int nt = 0; nt < 2; ++nt)
#pragma unroll
      for (int g = 0; g < 4; ++g) {
        f32x4 v = {acc[mt][nt][4 * g], acc[mt][nt][4 * g + 1], acc[mt][nt][4 * g + 2], acc[mt][nt][4 * g + 3]};
        *(f32x4*)(P + ((mt * 2 + nt) * 4 + g) * 256) = v;
      }
}

__global__ __launch_bounds__(256) void k3_reduce_ln(const u16* __restrict__ x, const u16* __restrict__ wr,
                                                    unsigned char* __restrict__ ws, u16* __restrict__ out) {
  __shared__ float red[3][4][8];
  const int tid = threadIdx.x, w = tid >> 6, l = tid & 63;
  const int m8 = l & 7, q = l >> 3, g = q >> 1, hh = q & 1;
  const int row = blockIdx.x * 8 + m8;
  const int mb = row >> 7, wm = (row >> 6) & 1, mt = (row >> 5) & 1, mr = row & 31;
  float hv[16];
  const float* P = (const float*)(ws + WS_PART);
  f32x4 pv[4][4];
  u32x2 xv[4], wv[4];
#pragma unroll
  for (int it = 0; it < 4; ++it) {
    const int T = w * 4 + it;
    const int wn = it >> 1, nt = it & 1;
#pragma unroll
    for (int ks = 0; ks < 4; ++ks) {
      const size_t off = ((((size_t)ks * 16 + mb) * 4 + w) * 4 + (wm * 2 + wn)) * 4096 + ((mt * 2 + nt) * 4 + g) * 256 + (hh * 32 + mr) * 4;
      pv[it][ks] = *(const f32x4*)(P + off);
    }
    const int n = T * 32 + 8 * g + 4 * hh;
    xv[it] = *(const u32x2*)(x + (size_t)row * D_ + n);
    wv[it] = *(const u32x2*)(wr + n);
  }
  float s = 0.f;
#pragma unroll
  for (int it = 0; it < 4; ++it) {
#pragma unroll
    for (int c = 0; c < 4; ++c) {
      const float y1 = rbf(pv[it][0][c] + pv[it][1][c]);
      const float y2 = rbf(-(pv[it][2][c] + pv[it][3][c]));
      const float y = rbf(y1 - y2);
      const float xx = (c & 1) ? bf_hi(xv[it][c >> 1]) : bf_lo(xv[it][c >> 1]);
      const float hq = rbf(xx + y);
      hv[it * 4 + c] = hq;
      s += hq;
    }
  }
  s += __shfl_xor(s, 8); s += __shfl_xor(s, 16); s += __shfl_xor(s, 32);
  if (l < 8) red[0][w][l] = s;
  __syncthreads();
  const float mean = ((red[0][0][m8] + red[0][1][m8]) + (red[0][2][m8] + red[0][3][m8])) * (1.0f / D_);
  float v = 0.f;
#pragma unroll
  for (int i = 0; i < 16; ++i) { const float d = hv[i] - mean; v = fmaf(d, d, v); }
  v += __shfl_xor(v, 8); v += __shfl_xor(v, 16); v += __shfl_xor(v, 32);
  if (l < 8) red[1][w][l] = v;
  __syncthreads();
  const float var = ((red[1][0][m8] + red[1][1][m8]) + (red[1][2][m8] + red[1][3][m8])) * (1.0f / D_);
  const float m_b = rbf(mean);
  const float sd = rbf(sqrtf(rbf(rbf(var) + 1e-5f)));
  float hn[16];
  float rr = 0.f;
#pragma unroll
  for (int it = 0; it < 4; ++it) {
#pragma unroll
    for (int c = 0; c < 4; ++c) {
      const float d = rbf(hv[it * 4 + c] - m_b);
      const float t = rbf(d / sd);
      hn[it * 4 + c] = t;
      const float ww = (c & 1) ? bf_hi(wv[it][c >> 1]) : bf_lo(wv[it][c >> 1]);
      rr = fmaf(t, ww, rr);
    }
  }
  rr += __shfl_xor(rr, 8); rr += __shfl_xor(rr, 16); rr += __shfl_xor(rr, 32);
  if (l < 8) red[2][w][l] = rr;
#pragma unroll
  for (int it = 0; it < 4; ++it) {
    const int n = (w * 4 + it) * 32 + 8 * g + 4 * hh;
    u32x2 o;
    o[0] = pk_bf16(hv[it * 4] + hn[it * 4], hv[it * 4 + 1] + hn[it * 4 + 1]);
    o[1] = pk_bf16(hv[it * 4 + 2] + hn[it * 4 + 2], hv[it * 4 + 3] + hn[it * 4 + 3]);
    *(u32x2*)(out + (size_t)row * D_ + n) = o;
  }
  __syncthreads();
  float best = -INFINITY; int bi = 0;
#pragma unroll
  for (int i = 0; i < 8; ++i) {
    const float ri = rbf((red[2][0][i] + red[2][1][i]) + (red[2][2][i] + red[2][3][i]));
    if (ri > best || i == 0) { best = ri; bi = i; }
  }
  if (m8 == bi) {
    u16* crow = (u16*)(ws + WS_CROW) + (size_t)blockIdx.x * 1024;
#pragma unroll
    for (int it = 0; it < 4; ++it) {
      const int n = (w * 4 + it) * 32 + 8 * g + 4 * hh;
      u32x2 o, o2;
      o[0] = pk_bf16(hn[it * 4], hn[it * 4 + 1]); o[1] = pk_bf16(hn[it * 4 + 2], hn[it * 4 + 3]);
      o2[0] = pk_bf16(hv[it * 4], hv[it * 4 + 1]); o2[1] = pk_bf16(hv[it * 4 + 2], hv[it * 4 + 3]);
      *(u32x2*)(crow + n) = o;
      *(u32x2*)(crow + 512 + n) = o2;
    }
  }
  if (tid == 0) {
    u32x2 c = {__float_as_uint(best), (unsigned)(blockIdx.x * 8 + bi)};
    *(u32x2*)(ws + WS_CAND + (size_t)blockIdx.x * 8) = c;
  }
}

DI void pick_winner(const unsigned char* ws, int l, float& rbest, int& wg) {
  float bv = -INFINITY; int bw = 0x7fffffff;
#pragma unroll
  for (int i = 0; i < 4; ++i) {
    const int c = i * 64 + l;
    const u32x2 cv = *(const u32x2*)(ws + WS_CAND + (size_t)c * 8);
    const float v = __uint_as_float(cv[0]);
    if (i == 0 || v > bv) { bv = v; bw = c; }
  }
#pragma unroll
  for (int o = 32; o >= 1; o >>= 1) {
    const float ov = __shfl_xor(bv, o); const int ow = __shfl_xor(bw, o);
    if (ov > bv || (ov == bv && ow < bw) || (bv != bv && (ov == ov || ow < bw))) { bv = ov; bw = ow; }
  }
  rbest = bv; wg = bw;
}

__global__ __launch_bounds__(256) void k4_gateup(const u16* __restrict__ wg_, const u16* __restrict__ wu_,
                                                 unsigned char* __restrict__ ws) {
  __shared__ float red[2][4][32];
  const int tid = threadIdx.x, w = tid >> 6, l = tid & 63;
  const int cg = l & 3, kg = (tid >> 2);
  const int f0 = blockIdx.x * 32 + cg * 8;
  u32x4 g[8], u[8];
#pragma unroll
  for (int i = 0; i < 8; ++i) {
    const int k = kg + 64 * i;
    g[i] = *(const u32x4*)(wg_ + (size_t)k * F_ + f0);
    u[i] = *(const u32x4*)(wu_ + (size_t)k * F_ + f0);
  }
  float rb; int win;
  pick_winner(ws, l, rb, win);
  const u16* hn = (const u16*)(ws + WS_CROW) + (size_t)win * 1024;
  float ag[8] = {}, au[8] = {};
#pragma unroll
  for (int i = 0; i < 8; ++i) {
    const float xv = __uint_as_float((unsigned)hn[kg + 64 * i] << 16);
#pragma unroll
    for (int q = 0; q < 4; ++q) {
      ag[2 * q] = fmaf(xv, bf_lo(g[i][q]), ag[2 * q]); ag[2 * q + 1] = fmaf(xv, bf_hi(g[i][q]), ag[2 * q + 1]);
      au[2 * q] = fmaf(xv, bf_lo(u[i][q]), au[2 * q]); au[2 * q + 1] = fmaf(xv, bf_hi(u[i][q]), au[2 * q + 1]);
    }
  }
#pragma unroll
  for (int c = 0; c < 8; ++c) {
#pragma unroll
    for (int o = 4; o <= 32; o <<= 1) { ag[c] += __shfl_xor(ag[c], o); au[c] += __shfl_xor(au[c], o); }
  }
  if (l < 4) {
#pragma unroll
    for (int c = 0; c < 8; ++c) { red[0][w][l * 8 + c] = ag[c]; red[1][w][l * 8 + c] = au[c]; }
  }
  __syncthreads();
  if (tid < 32) {
    const float gs = rbf((red[0][0][tid] + red[0][1][tid]) + (red[0][2][tid] + red[0][3][tid]));
    const float us = rbf((red[1][0][tid] + red[1][1][tid]) + (red[1][2][tid] + red[1][3][tid]));
    const float sg = rbf(gs / (1.0f + __expf(-gs)));
    ((float*)(ws + WS_ACT))[blockIdx.x * 32 + tid] = rbf(sg * us);
  }
}

__global__ __launch_bounds__(256) void k5_down(const u16* __restrict__ wd_, unsigned char* __restrict__ ws,
                                               u16* __restrict__ out) {
  __shared__ float red[4][16];
  const int tid = threadIdx.x, w = tid >> 6, l = tid & 63;
  const int cg = l & 1, kg = tid >> 1;
  const int e0 = blockIdx.x * 16 + cg * 8;
  u32x4 d[14];
#pragma unroll
  for (int i = 0; i < 14; ++i) d[i] = *(const u32x4*)(wd_ + (size_t)(kg + 128 * i) * D_ + e0);
  const float* act = (const float*)(ws + WS_ACT);
  float a[8] = {};
#pragma unroll
  for (int i = 0; i < 14; ++i) {
    const float xv = act[kg + 128 * i];
#pragma unroll
    for (int q = 0; q < 4; ++q) { a[2 * q] = fmaf(xv, bf_lo(d[i][q]), a[2 * q]); a[2 * q + 1] = fmaf(xv, bf_hi(d[i][q]), a[2 * q + 1]); }
  }
  float rb; int win;
  pick_winner(ws, l, rb, win);
#pragma unroll
  for (int c = 0; c < 8; ++c) {
#pragma unroll
    for (int o = 2; o <= 32; o <<= 1) a[c] += __shfl_xor(a[c], o);
  }
  if (l < 2) {
#pragma unroll
    for (int c = 0; c < 8; ++c) red[w][l * 8 + c] = a[c];
  }
  __syncthreads();
  if (tid < 16) {
    const u32x2 cv = *(const u32x2*)(ws + WS_CAND + (size_t)win * 8);
    const int row = (int)cv[1];
    const u16* crow = (const u16*)(ws + WS_CROW) + (size_t)win * 1024;
    const int e = blockIdx.x * 16 + tid;
    const float y = rbf((red[0][tid] + red[1][tid]) + (red[2][tid] + red[3][tid]));
    const float rs = rbf(1.0f / (1.0f + __expf(-rb)));
    const float y2 = rbf(y * rs);
    const float hn = __uint_as_float((unsigned)crow[e] << 16), hh = __uint_as_float((unsigned)crow[512 + e] << 16);
    const float o = rbf(hn + y2);
    out[(size_t)row * D_ + e] = (u16)(pk_bf16(hh + o, 0.f) & 0xffffu);
  }
}

extern "C" void kernel_launch(void* const* d_in, const int* in_sizes, int n_in, void* d_out, int out_size, void* d_ws,
                              size_t ws_size, hipStream_t stream) {
  const u16* x = (const u16*)d_in[0];
  const u16* cr = (const u16*)d_in[1];
  const u16* ci = (const u16*)d_in[2];
  const u16* dr = (const u16*)d_in[3];
  const u16* di = (const u16*)d_in[4];
  const u16* wr = (const u16*)d_in[6];
  const u16* wg = (const u16*)d_in[7];
  const u16* wu = (const u16*)d_in[8];
  const u16* wd = (const u16*)d_in[9];
  unsigned char* ws = (unsigned char*)d_ws;
  u16* out = (u16*)d_out;
  (void)hipFuncSetAttribute((const void*)k2_gemm, hipFuncAttributeMaxDynamicSharedMemorySize, K2_NST * 32768);
  k1_ln_gemm<<<256, 256, 0, stream>>>(x, dr, di, ws);
  k2_gemm<<<256, 256, K2_NST * 32768, stream>>>(cr, ci, ws);
  k3_reduce_ln<<<256, 256, 0, stream>>>(x, wr, ws, out);
  k4_gateup<<<56, 256, 0, stream>>>(wg, wu, ws);
  k5_down<<<32, 256, 0, stream>>>(wd, ws, out);
}
```

```cpp
#include <hip/hip_runtime.h>
#include <stdint.h>

typedef unsigned short u16;
typedef short bf16x8 __attribute__((ext_vector_type(8)));
typedef __bf16 bf16x2_t __attribute__((ext_vector_type(2)));
typedef float f32x16 __attribute__((ext_vector_type(16)));
typedef float f32x4 __attribute__((ext_vector_type(4)));
typedef float f32x2 __attribute__((ext_vector_type(2)));
typedef unsigned u32x4 __attribute__((ext_vector_type(4)));
typedef unsigned u32x2 __attribute__((ext_vector_type(2)));
typedef unsigned long long u64;
#define DI __device__ __forceinline__
#define LAS __attribute__((address_space(3)))
#define GAS __attribute__((address_space(1)))
#define MFMA32(a, b, c) __builtin_amdgcn_mfma_f32_32x32x16_bf16((a), (b), (c), 0, 0, 0)
#define MFMA16(a, b, c) __builtin_amdgcn_mfma_f32_16x16x32_bf16((a), (b), (c), 0, 0, 0)
#define DMA(rs, ldsp, voff, soff, imm, aux) __builtin_amdgcn_raw_ptr_buffer_load_lds(rs, (LAS void*)(ldsp), 16, voff, soff, imm, aux)
#define WAITV(n) asm volatile("s_waitcnt vmcnt(" #n ")" ::: "memory")
#define WAITL() asm volatile("s_waitcnt lgkmcnt(0)" ::: "memory")
#define BAR() do { asm volatile("" ::: "memory"); __builtin_amdgcn_s_barrier(); asm volatile("" ::: "memory"); } while (0)
#define SB() __builtin_amdgcn_sched_barrier(0)
#define RLX_AGENT __ATOMIC_RELAXED, __HIP_MEMORY_SCOPE_AGENT
#define RSRC(p, bytes) __builtin_amdgcn_make_buffer_rsrc((void*)(p), (short)0, (int)(bytes), 0x00020000)
constexpr int AUX_NT = 2, AUX_SC1 = 16;
#define K1AUX 2
#define K1PF 1
#define K2TOUCH 1

constexpr int S_ = 2048, D_ = 512, F_ = 1792;
#define TSDECL
#define STAMP(i)
#define TSDUMP(kern, wave)

DI float bf_lo(unsigned w) { return __uint_as_float(w << 16); }
DI float bf_hi(unsigned w) { return __uint_as_float(w & 0xffff0000u); }
DI unsigned pk_bf16(float lo, float hi) {
  f32x2 v = {lo, hi};
  bf16x2_t b = __builtin_convertvector(v, bf16x2_t);
  return __builtin_bit_cast(unsigned, b);
}
DI float dot2(unsigned a, unsigned b, float acc) {
  return __builtin_amdgcn_fdot2_f32_bf16(__builtin_bit_cast(bf16x2_t, a), __builtin_bit_cast(bf16x2_t, b), acc, false);
}
template <int CTRL> DI float dpp(float x) {
  return __builtin_bit_cast(float, __builtin_amdgcn_mov_dpp(__builtin_bit_cast(int, x), CTRL, 0xf, 0xf, true));
}
template <int CTRL> DI unsigned dppu(unsigned x) { return (unsigned)__builtin_amdgcn_mov_dpp((int)x, CTRL, 0xf, 0xf, true); }
constexpr int XOR1 = 0xB1, XOR2 = 0x4E, XOR7 = 0x141, XOR8 = 0x128;
DI float fold32(float a, float b) {
  auto t = __builtin_amdgcn_permlane32_swap(__float_as_uint(a), __float_as_uint(b), false, false);
  return __uint_as_float(t[0]) + __uint_as_float(t[1]);
}
DI float fold16(float a, float b) {
  auto t = __builtin_amdgcn_permlane16_swap(__float_as_uint(a), __float_as_uint(b), false, false);
  return __uint_as_float(t[0]) + __uint_as_float(t[1]);
}
DI float bsel(unsigned m, float a, float b) { return __uint_as_float((m & __float_as_uint(a)) | (~m & __float_as_uint(b))); }
DI unsigned ord_f32(float v) { const unsigned u = __float_as_uint(v); return u ^ ((unsigned)((int)u >> 31) | 0x80000000u); }
DI float unord_f32(unsigned o) { return __uint_as_float(o ^ ((o >> 31) ? 0x80000000u : 0xffffffffu)); }
DI void max2(unsigned& hi, unsigned& lo, unsigned ohi, unsigned olo) {
  const bool t = (ohi > hi) | ((ohi == hi) & (olo > lo));
  hi = t ? ohi : hi; lo = t ? olo : lo;
}

constexpr size_t WS_AB = 0;
constexpr size_t WS_PART = WS_AB + (size_t)4096 * 512 * 2;
constexpr size_t WS_BASE = WS_PART + (size_t)256 * 32768;
constexpr size_t WS_FLAG = WS_BASE + (size_t)128 * 2048;
constexpr int FL_HN = 0;
constexpr int FL_Y2 = 128 * 2048;
constexpr int FL_ARR = FL_Y2 + 32 * 56 * 64;
constexpr int FL_CAND = FL_ARR + 128 * 128;
constexpr int FL_WIN = FL_CAND + 8 * 1024;
constexpr int FL_DONE = FL_WIN + 32 * 128;
constexpr int FL_PER = 1664;
constexpr int FL_BYTES = 256 * FL_PER;
static_assert(FL_DONE + 512 <= FL_BYTES, "flag area");
constexpr unsigned SPIN_CAP = 1u << 18;
#define TMO(code)

#define ABAUX 0
#define WAUX 0
#define WPF 0
constexpr int K1_BUF = 24576, K1_ST = 6 * K1_BUF, K1_SD = K1_ST + 2048, K1_PF = K1_SD + 256, K1_LDS = K1_PF + 2048;
__global__ __launch_bounds__(512, 2) void k1_ln_gemm(const u16* __restrict__ x, const u16* __restrict__ dr,
                                                     const u16* __restrict__ di, const u16* __restrict__ cr,
                                                     const u16* __restrict__ ci, const u16* __restrict__ wg_,
                                                     const u16* __restrict__ wu_, const u16* __restrict__ wd_,
                                                     unsigned char* ws) {
  extern __shared__ __attribute__((aligned(16))) unsigned char smem[];
  LAS unsigned char* L = (LAS unsigned char*)smem;
  const int tid = threadIdx.x, w8 = __builtin_amdgcn_readfirstlane(tid >> 6), w = w8 & 3, l = tid & 63, r = l & 31, h = l >> 5;
  const int b = blockIdx.x, xcd = b & 7, j = b >> 3;
  TSDECL STAMP(0);
  const int isB = xcd >> 2, sb = ((xcd >> 1) & 1) * 8 + (xcd & 1) * 4 + (j & 3), e8 = j >> 2;
  const u16* dm = (isB ? di : dr) + (size_t)e8 * 64 * D_;
  const auto rx = RSRC(x + (size_t)sb * 128 * D_, 128 * 1024);
  const auto rd = RSRC(dm, 64 * 1024);
  const int v0 = (l >> 3) * 1024 + (((l & 7) ^ ((l >> 4) & 7)) * 16);
  const int v1 = (l >> 3) * 1024 + (((l & 7) ^ ((4 + (l >> 4)) & 7)) * 16) + 7 * 1024;
  const int rot = (e8 + 2 * (j & 3)) & 7;
  auto issue = [&](int t_, int buf) {
    const int ko = ((t_ + rot) & 7) * 128;
    LAS unsigned char* bx = L + buf * K1_BUF + w * 4096;
    LAS unsigned char* bd = L + buf * K1_BUF + 16384 + w * 2048;
    DMA(rd, bd, v0, 16 * w * 1024 + ko, 0, K1AUX);
    DMA(rd, bd, v1, 16 * w * 1024 + ko, 1024, K1AUX);
    DMA(rx, bx, v0, 32 * w * 1024 + ko, 0, K1AUX);
    DMA(rx, bx, v1, 32 * w * 1024 + ko, 1024, K1AUX);
    DMA(rx, bx, v0, 32 * w * 1024 + ko + 14 * 1024, 2048, K1AUX);
    DMA(rx, bx, v1, 32 * w * 1024 + ko + 14 * 1024, 3072, K1AUX);
  };
  issue(w8 >> 2, w8 >> 2);
  STAMP(1);
  const int i16 = l & 15, q4 = l >> 4;
  if (w8 >= 4) {
    issue(2, 2);
    f32x4 sdv = {};
    const bf16x8 ones = {0x3f80, 0x3f80, 0x3f80, 0x3f80, 0x3f80, 0x3f80, 0x3f80, 0x3f80};
    const int sdd = 16384 + (w * 16 + i16) * 128;
    const int ps0 = (q4 ^ (i16 >> 1)) * 16, ps1 = ((4 + q4) ^ (i16 >> 1)) * 16;
#pragma unroll
    for (int t = 0; t < 8; ++t) {
      if (t >= 1 && t < 5) WAITV(12); else if (t == 5) WAITV(15); else if (t == 6) WAITV(9); else if (t == 7) WAITV(3);
      if (t == 1) STAMP(12); if (t == 2) STAMP(13); if (t == 6) STAMP(14); if (t == 7) STAMP(15);
      WAITL();
      BAR();
      const LAS unsigned char* bb = L + (t % 6) * K1_BUF;
      const bf16x8 d0 = *(const LAS bf16x8*)(bb + sdd + ps0), d1 = *(const LAS bf16x8*)(bb + sdd + ps1);
      SB();
      if (t < 5) issue(t + 3, (t + 3) % 6);
      if (t == 4) {
        const int ks = xcd >> 1;
        const u16* A = ((ks >> 1) ? ci : cr) + (size_t)((xcd & 1) * 1024 + j * 32) * S_ + (ks & 1) * 1024;
        const auto rp = RSRC(A, K1PF ? 31 * 4096 + 2048 : 0);
        const int vo = (w * 8 + (l >> 4)) * 4096 + (l & 15) * 128;
        __builtin_amdgcn_raw_ptr_buffer_load_lds(rp, (LAS void*)(L + K1_PF + w * 512), 4, vo, 0, 0, AUX_NT);
        __builtin_amdgcn_raw_ptr_buffer_load_lds(rp, (LAS void*)(L + K1_PF + w * 512 + 256), 4, vo, 4 * 4096, 0, AUX_NT);
        const u16* wp = w == 0 ? wg_ : w == 1 ? wu_ : wd_ + (size_t)(xcd * 224 + j * 7) * D_;
        const auto rq = RSRC(wp, !WPF ? 0 : w < 2 ? D_ * F_ * 2 : w == 2 ? 7 * D_ * 2 : 0);
        const int vw = w < 2 ? (j * 16 + (l >> 2)) * (F_ * 2) + ((xcd * 448) & ~127) + (l & 3) * 128 : l * 128;
        __builtin_amdgcn_raw_ptr_buffer_load_lds(rq, (LAS void*)(L + K1_PF + w * 512), 4, vw, 0, 0, WAUX);
        const int t4 = tid - 256;
        if (t4 < FL_PER / 16) *(u32x4*)(ws + WS_FLAG + (size_t)b * FL_PER + t4 * 16) = (u32x4){0u, 0u, 0u, 0u};
      }
      SB();
      sdv = MFMA16(d0, ones, sdv);
      sdv = MFMA16(d1, ones, sdv);
      SB();
    }
    if (i16 == 0) *(LAS f32x4*)(L + K1_SD + (w * 16 + q4 * 4) * 4) = sdv;
    WAITL();
    BAR();
    STAMP(6);
    STAMP(8); TSDUMP(0, w8);
    return;
  }
  f32x16 acc0 = {}, acc1 = {}, gram = {};
  f32x4 rsum = {};
  const short sv = (i16 == (q4 & 1)) ? (short)0x3f80 : (short)0;
  const bf16x8 selB = {sv, sv, sv, sv, sv, sv, sv, sv};
  bf16x8 fx[2][4], f0[2][4], f1[2][4];
  int pos[4];
#pragma unroll
  for (int kk = 0; kk < 4; ++kk) pos[kk] = r * 128 + (((2 * kk + h) ^ ((r >> 1) & 7)) * 16);
  auto ldf = [&](int t, int kk) {
    const LAS unsigned char* bb = L + (t % 6) * K1_BUF;
    fx[t & 1][kk] = *(const LAS bf16x8*)(bb + w * 4096 + pos[kk]);
    f0[t & 1][kk] = *(const LAS bf16x8*)(bb + 16384 + pos[kk]);
    f1[t & 1][kk] = *(const LAS bf16x8*)(bb + 16384 + 4096 + pos[kk]);
  };
  WAITV(0);
  BAR();
  STAMP(2);
#pragma unroll
  for (int kk = 0; kk < 4; ++kk) ldf(0, kk);
#pragma unroll
  for (int t = 0; t < 7; ++t) {
    const int p = t & 1;
    WAITL();
    BAR();
    if (t == 3) STAMP(3);
#pragma unroll
    for (int kk = 0; kk < 4; ++kk) {
      ldf(t + 1, kk);
      SB();
      gram = MFMA32(fx[p][kk], fx[p][kk], gram);
      rsum = MFMA16(fx[p][kk], selB, rsum);
      acc0 = MFMA32(fx[p][kk], f0[p][kk], acc0);
      acc1 = MFMA32(fx[p][kk], f1[p][kk], acc1);
      SB();
    }
  }
#pragma unroll
  for (int kk = 0; kk < 4; ++kk) { gram = MFMA32(fx[1][kk], fx[1][kk], gram); rsum = MFMA16(fx[1][kk], selB, rsum); }
  SB();
  acc0 = MFMA32(fx[1][0], f0[1][0], acc0); acc1 = MFMA32(fx[1][0], f1[1][0], acc1);
  acc0 = MFMA32(fx[1][1], f0[1][1], acc0); acc1 = MFMA32(fx[1][1], f1[1][1], acc1);
  SB();
  LAS float* st = (LAS float*)(L + K1_ST + w * 512);
  if (i16 < 2) *(LAS f32x4*)(st + i16 * 16 + q4 * 4) = rsum;
  float dg;
  {
    const unsigned m0 = 0u - (unsigned)(r & 1), m1 = 0u - (unsigned)((r >> 1) & 1), m3 = 0u - (unsigned)((r >> 3) & 1), m4 = 0u - (unsigned)(r >> 4);
    float e[4];
#pragma unroll
    for (int g = 0; g < 4; ++g) e[g] = bsel(m1, bsel(m0, gram[4 * g + 3], gram[4 * g + 2]), bsel(m0, gram[4 * g + 1], gram[4 * g]));
    dg = bsel(m4, bsel(m3, e[3], e[2]), bsel(m3, e[1], e[0]));
  }
  SB();
  acc0 = MFMA32(fx[1][2], f0[1][2], acc0); acc1 = MFMA32(fx[1][2], f1[1][2], acc1);
  SB();
  {
    const float mean = st[r] * (1.0f / D_);
    float rs = __builtin_amdgcn_rsqf(fmaxf(dg * (1.0f / D_) - mean * mean, 0.f) + 1e-5f);
    if (isB) rs = -rs;
    if (h == ((r >> 2) & 1)) *(LAS f32x2*)(st + 32 + 2 * r) = (f32x2){mean * rs, rs};
  }
  SB();
  acc0 = MFMA32(fx[1][3], f0[1][3], acc0); acc1 = MFMA32(fx[1][3], f1[1][3], acc1);
  SB();
  STAMP(6);
  WAITL();
  BAR();
  const LAS float* sdl = (const LAS float*)(L + K1_SD);
  const float cs0 = sdl[r], cs1 = sdl[32 + r];
  const LAS f32x4* st4 = (const LAS f32x4*)(st + 32);
  unsigned p0[8], p1[8];
#pragma unroll
  for (int g = 0; g < 4; ++g) {
    const f32x4 sa = st4[4 * g + 2 * h], sbv = st4[4 * g + 2 * h + 1];
    p0[2 * g] = pk_bf16(acc0[4 * g] * sa[1] - sa[0] * cs0, acc0[4 * g + 1] * sa[3] - sa[2] * cs0);
    p0[2 * g + 1] = pk_bf16(acc0[4 * g + 2] * sbv[1] - sbv[0] * cs0, acc0[4 * g + 3] * sbv[3] - sbv[2] * cs0);
    p1[2 * g] = pk_bf16(acc1[4 * g] * sa[1] - sa[0] * cs1, acc1[4 * g + 1] * sa[3] - sa[2] * cs1);
    p1[2 * g + 1] = pk_bf16(acc1[4 * g + 2] * sbv[1] - sbv[0] * cs1, acc1[4 * g + 3] * sbv[3] - sbv[2] * cs1);
  }
  const int kk0 = isB * 2048 + sb * 128 + w * 32;
  const int kb = kk0 >> 6, kst = (kk0 >> 4) & 3;
  const int n0 = e8 * 64, nb = n0 >> 7, nt0 = (n0 >> 5) & 3;
  const auto rab = RSRC(ws + WS_AB + (size_t)(nb * 64 + kb) * 16384, 16384);
#pragma unroll
  for (int s2 = 0; s2 < 2; ++s2) {
    {
      auto t0 = __builtin_amdgcn_permlane32_swap(p0[4 * s2], p0[4 * s2 + 2], false, false);
      auto t1 = __builtin_amdgcn_permlane32_swap(p0[4 * s2 + 1], p0[4 * s2 + 3], false, false);
      u32x4 o = {t0[0], t1[0], t0[1], t1[1]};
      __builtin_amdgcn_raw_buffer_store_b128(o, rab, l * 16, ((kst + s2) * 4 + nt0) * 1024, ABAUX);
    }
    {
      auto t0 = __builtin_amdgcn_permlane32_swap(p1[4 * s2], p1[4 * s2 + 2], false, false);
      auto t1 = __builtin_amdgcn_permlane32_swap(p1[4 * s2 + 1], p1[4 * s2 + 3], false, false);
      u32x4 o = {t0[0], t1[0], t0[1], t1[1]};
      __builtin_amdgcn_raw_buffer_store_b128(o, rab, l * 16, ((kst + s2) * 4 + nt0 + 1) * 1024, ABAUX);
    }
  }
  STAMP(7);
  STAMP(8); TSDUMP(0, w8);
}

constexpr int SC_RED = 4 * 32768;
constexpr int SC_X = SC_RED + 2048;
constexpr int SC_GU = SC_X + 2048;
constexpr int SC_ACT = SC_GU + 2048;
constexpr int K2_LDS = SC_ACT + 1024;

DI void wait_winner(unsigned char* ws, GAS u64* cand, int l, unsigned& bhi, unsigned& blo) {
  for (unsigned spins = 0;; ++spins) {
    const u64 g0 = __hip_atomic_load(cand + l, RLX_AGENT), g1 = __hip_atomic_load(cand + 64 + l, RLX_AGENT);
    bhi = (unsigned)(g0 >> 32); blo = (unsigned)g0;
    max2(bhi, blo, (unsigned)(g1 >> 32), (unsigned)g1);
    if (__all((((unsigned)g0 & (unsigned)g1) >> 31) != 0u) || spins > SPIN_CAP) { TMO(1); break; }
    __builtin_amdgcn_s_sleep(2);
  }
  max2(bhi, blo, dppu<XOR1>(bhi), dppu<XOR1>(blo));
  max2(bhi, blo, dppu<XOR2>(bhi), dppu<XOR2>(blo));
  max2(bhi, blo, dppu<XOR7>(bhi), dppu<XOR7>(blo));
  max2(bhi, blo, dppu<XOR8>(bhi), dppu<XOR8>(blo));
  {
    auto th = __builtin_amdgcn_permlane16_swap(bhi, bhi, false, false);
    auto tl = __builtin_amdgcn_permlane16_swap(blo, blo, false, false);
    bhi = th[0]; blo = tl[0]; max2(bhi, blo, th[1], tl[1]);
  }
  {
    auto th = __builtin_amdgcn_permlane32_swap(bhi, bhi, false, false);
    auto tl = __builtin_amdgcn_permlane32_swap(blo, blo, false, false);
    bhi = th[0]; blo = tl[0]; max2(bhi, blo, th[1], tl[1]);
  }
}

__global__ __launch_bounds__(512, 2) void k2_gemm(const u16* __restrict__ cr, const u16* __restrict__ ci,
                                                  const u16* __restrict__ x, const u16* __restrict__ wr,
                                                  const u16* __restrict__ wg_, const u16* __restrict__ wu_,
                                                  const u16* __restrict__ wd_, unsigned char* ws,
                                                  u16* out) {
  extern __shared__ __attribute__((aligned(16))) unsigned char smem[];
  LAS unsigned char* L = (LAS unsigned char*)smem;
  const int tid = threadIdx.x, w8 = __builtin_amdgcn_readfirstlane(tid >> 6), w = w8 & 3, l = tid & 63, r = l & 31, h = l >> 5;
  const int b = blockIdx.x, xcd = b & 7, j = b >> 3;
  const int ks = xcd >> 1, mb = (xcd & 1) * 8 + (j >> 2), nb = j & 3;
  TSDECL STAMP(0);
  const u16* A = ((ks >> 1) ? ci : cr) + (size_t)mb * 128 * S_ + (ks & 1) * 1024;
  const auto ra = RSRC(A, 127 * 4096 + 2048);
  const auto rb = RSRC(ws + WS_AB + (size_t)(nb * 64 + ks * 16) * 16384, 16 * 16384);
  const int va0 = (32 * w + (l >> 3)) * 4096 + (((l & 7) ^ ((l >> 4) & 7)) * 16);
  const int va1 = (32 * w + (l >> 3)) * 4096 + (((l & 7) ^ ((4 + (l >> 4)) & 7)) * 16);
  const int vb = w * 4096 + l * 16;
  auto stage = [&](int t, int buf) {
    DMA(ra, L + buf * 32768 + w * 4096, va0, t * 128, 0, 0);
    DMA(ra, L + buf * 32768 + w * 4096, va1, t * 128 + 1 * (32768 - 1024), 1024, 0);
    DMA(ra, L + buf * 32768 + w * 4096, va0, t * 128 + 2 * (32768 - 1024), 2048, 0);
    DMA(ra, L + buf * 32768 + w * 4096, va1, t * 128 + 3 * (32768 - 1024), 3072, 0);
    DMA(rb, L + buf * 32768 + 16384 + w * 4096, vb, t * 16384, 0, 0);
    DMA(rb, L + buf * 32768 + 16384 + w * 4096, vb, t * 16384, 1024, 0);
    DMA(rb, L + buf * 32768 + 16384 + w * 4096, vb, t * 16384, 2048, 0);
    DMA(rb, L + buf * 32768 + 16384 + w * 4096, vb, t * 16384, 3072, 0);
  };
  stage(w8 >> 2, w8 >> 2);
  if (w8 >= 4) {
    BAR();
    stage(2, 2); stage(3, 3);
#pragma unroll
    for (int t = 0; t < 12; ++t) {
      WAITV(16);
      BAR();
      stage(t + 4, t & 3);
    }
    WAITV(16); BAR(); WAITV(8); BAR(); WAITV(0); BAR();
    STAMP(2);
  } else {
    const int wm = w >> 1, wn = w & 1;
    f32x16 acc[2][2] = {};
    int aoff[4];
    const int sw = (r >> 1) & 7;
#pragma unroll
    for (int kk = 0; kk < 4; ++kk) aoff[kk] = (wm * 64 + r) * 128 + (((2 * kk + h) ^ sw) * 16);
    const int boff = 16384 + wn * 2048 + l * 16;
    bf16x8 fa[2][2], fb[2][2];
    auto ldf = [&](int t, int kk, int p) {
      const int bo = (t & 3) * 32768;
      fb[p][0] = *(const LAS bf16x8*)(L + bo + boff + kk * 4096);
      fa[p][0] = *(const LAS bf16x8*)(L + bo + aoff[kk]);
      fa[p][1] = *(const LAS bf16x8*)(L + bo + aoff[kk] + 4096);
      fb[p][1] = *(const LAS bf16x8*)(L + bo + boff + kk * 4096 + 1024);
    };
    auto mma = [&](int p) {
      acc[0][0] = MFMA32(fb[p][0], fa[p][0], acc[0][0]);
      acc[1][0] = MFMA32(fb[p][0], fa[p][1], acc[1][0]);
      acc[0][1] = MFMA32(fb[p][1], fa[p][0], acc[0][1]);
      acc[1][1] = MFMA32(fb[p][1], fa[p][1], acc[1][1]);
      SB();
    };
    WAITV(0);
    BAR();
    STAMP(1);
    ldf(0, 0, 0);
#pragma unroll
    for (int t = 0; t < 16; ++t) {
      ldf(t, 1, 1); SB(); mma(0);
      ldf(t, 2, 0); SB(); mma(1);
      ldf(t, 3, 1); SB(); mma(0);
      if (t < 15) { WAITL(); BAR(); ldf(t + 1, 0, 0); SB(); }
      mma(1);
    }
    STAMP(2);
    const auto rp = RSRC(ws + WS_PART + (size_t)((ks * 16 + mb) * 4 + nb) * 32768, 32768);
#pragma unroll
    for (int nt = 0; nt < 2; ++nt)
#pragma unroll
      for (int mt = 0; mt < 2; ++mt)
#pragma unroll
        for (int s2 = 0; s2 < 2; ++s2) {
          const f32x16& a = acc[mt][nt];
          const unsigned p0 = pk_bf16(a[8 * s2], a[8 * s2 + 1]), p1 = pk_bf16(a[8 * s2 + 2], a[8 * s2 + 3]);
          const unsigned p2 = pk_bf16(a[8 * s2 + 4], a[8 * s2 + 5]), p3 = pk_bf16(a[8 * s2 + 6], a[8 * s2 + 7]);
          auto t0 = __builtin_amdgcn_permlane32_swap(p0, p2, false, false);
          auto t1 = __builtin_amdgcn_permlane32_swap(p1, p3, false, false);
          const u32x4 o = {t0[0], t1[0], t0[1], t1[1]};
          __builtin_amdgcn_raw_buffer_store_b128(o, rp, (((w * 2 + mt) * 2 + nt) * 2 + s2) * 1024 + l * 16, 0, AUX_SC1);
          SB();
        }
    STAMP(3);
    WAITV(0);
    if (l < 4)
      __hip_atomic_store((GAS unsigned*)(ws + WS_FLAG + FL_ARR) + (mb * 8 + wm * 4 + l) * 32 + (ks * 4 + nb) * 2 + wn, 1u, RLX_AGENT);
    STAMP(4);
  }
  GAS u64* cand = (GAS u64*)(ws + WS_FLAG + FL_CAND);
  const auto ro = RSRC(out, S_ * D_ * 2);
  const auto rbase = RSRC(ws + WS_BASE, 128 * 2048);

  if ((j & 1) == 0) {
    const int q2 = ks * 2 + (nb >> 1), ci_ = mb * 8 + q2;
    const int r16 = l & 15, c = tid >> 4;
    const int row = mb * 128 + q2 * 16 + r16;
    BAR();
    u32x4 xv[2], wv[2];
#pragma unroll
    for (int u = 0; u < 2; ++u) {
      xv[u] = __builtin_nontemporal_load((const u32x4*)(x + (size_t)row * D_ + c * 8 + u * 256));
      wv[u] = *(const u32x4*)(wr + c * 8 + u * 256);
    }
    if (w8 == 5) {
      GAS unsigned* arr = (GAS unsigned*)(ws + WS_FLAG + FL_ARR) + ci_ * 32 + r;
      for (unsigned spins = 0;; ++spins) {
        if (__all(__hip_atomic_load(arr, RLX_AGENT) != 0u) || spins > SPIN_CAP) { TMO(0); break; }
        __builtin_amdgcn_s_sleep(1);
      }
    }
    const auto rp = RSRC(ws + WS_PART, 256 * 32768);
    const int poff = (mb * 4 + (c >> 4)) * 32768 +
                     (((((q2 >> 2) * 2 + ((c >> 3) & 1)) * 2 + ((q2 >> 1) & 1)) * 2 + ((c >> 2) & 1)) * 2 + ((c >> 1) & 1)) * 1024 +
                     ((c & 1) * 32 + (q2 & 1) * 16 + r16) * 16;
    SB();
    BAR();
    STAMP(5);
    u32x4 pv[2][4];
#pragma unroll
    for (int u = 0; u < 2; ++u)
#pragma unroll
      for (int k2 = 0; k2 < 4; ++k2) pv[u][k2] = __builtin_amdgcn_raw_buffer_load_b128(rp, poff, (k2 * 64 + u * 2) * 32768, AUX_SC1);
    float sw = 0.f, wf[16], hv[16];
#pragma unroll
    for (int u = 0; u < 2; ++u)
#pragma unroll
      for (int i = 0; i < 4; ++i) {
        wf[8 * u + 2 * i] = bf_lo(wv[u][i]); wf[8 * u + 2 * i + 1] = bf_hi(wv[u][i]);
        sw += wf[8 * u + 2 * i] + wf[8 * u + 2 * i + 1];
        hv[8 * u + 2 * i] = bf_lo(xv[u][i]) + ((bf_lo(pv[u][0][i]) + bf_lo(pv[u][1][i])) + (bf_lo(pv[u][2][i]) + bf_lo(pv[u][3][i])));
        hv[8 * u + 2 * i + 1] = bf_hi(xv[u][i]) + ((bf_hi(pv[u][0][i]) + bf_hi(pv[u][1][i])) + (bf_hi(pv[u][2][i]) + bf_hi(pv[u][3][i])));
      }
    float mu = 0.f, m2 = 0.f, hw = 0.f;
#pragma unroll
    for (int i = 0; i < 4; ++i) mu += (hv[4 * i] + hv[4 * i + 1]) + (hv[4 * i + 2] + hv[4 * i + 3]);
    mu *= 0.0625f;
    STAMP(6);
#pragma unroll
    for (int i = 0; i < 16; ++i) { const float d = hv[i] - mu; m2 = fmaf(d, d, m2); hw = fmaf(hv[i], wf[i], hw); }
    float hs = fold16(hw, sw);
    {
      auto t = __builtin_amdgcn_permlane16_swap(__float_as_uint(mu), __float_as_uint(mu), false, false);
      const float a = __uint_as_float(t[0]), bb = __uint_as_float(t[1]), d = a - bb;
      m2 = fold16(m2, m2) + d * d * 8.0f; mu = (a + bb) * 0.5f;
    }
    hs = fold32(hs, hs);
    {
      auto t = __builtin_amdgcn_permlane32_swap(__float_as_uint(mu), __float_as_uint(mu), false, false);
      const float a = __uint_as_float(t[0]), bb = __uint_as_float(t[1]), d = a - bb;
      m2 = fold32(m2, m2) + d * d * 16.0f; mu = (a + bb) * 0.5f;
    }
    LAS float* red = (LAS float*)(L + SC_RED);
    if (l < 16) *(LAS f32x2*)(red + (l * 8 + w8) * 4) = (f32x2){mu, m2};
    if (l < 32) red[(r16 * 8 + w8) * 4 + 2 + (l >> 4)] = hs;
    WAITL();
    BAR();
    float mean = 0.f, M2 = 0.f, HW = 0.f, SW = 0.f;
    {
      f32x4 t[8];
#pragma unroll
      for (int i = 0; i < 8; ++i) t[i] = *(const LAS f32x4*)(red + (r16 * 8 + i) * 4);
#pragma unroll
      for (int i = 0; i < 8; ++i) { mean += t[i][0]; M2 += t[i][1]; HW += t[i][2]; SW += t[i][3]; }
      mean *= 0.125f;
#pragma unroll
      for (int i = 0; i < 8; ++i) { const float d = t[i][0] - mean; M2 = fmaf(d * d, 64.0f, M2); }
    }
    const float rstd = __builtin_amdgcn_rsqf(M2 * (1.0f / D_) + 1e-5f);
    const float rr = (HW - mean * SW) * rstd;
    unsigned khi = ord_f32(rr), klo = 0x80000000u | (unsigned)((2047 - row) << 8) | (unsigned)ci_;
    max2(khi, klo, dppu<XOR1>(khi), dppu<XOR1>(klo));
    max2(khi, klo, dppu<XOR2>(khi), dppu<XOR2>(klo));
    max2(khi, klo, dppu<XOR7>(khi), dppu<XOR7>(klo));
    max2(khi, klo, dppu<XOR8>(khi), dppu<XOR8>(klo));
    if (tid < 8) __hip_atomic_store(cand + tid * 128 + ci_, ((u64)khi << 32) | (u64)klo, RLX_AGENT);
    STAMP(7);
    const bool best = ((2047 - (int)((klo >> 8) & 0x7ffu)) == row);
#pragma unroll
    for (int u = 0; u < 2; ++u) {
      float hn[8], ob[8];
#pragma unroll
      for (int i = 0; i < 8; ++i) { hn[i] = (hv[8 * u + i] - mean) * rstd; ob[i] = hv[8 * u + i] + hn[i]; }
      const u32x4 o = {pk_bf16(ob[0], ob[1]), pk_bf16(ob[2], ob[3]), pk_bf16(ob[4], ob[5]), pk_bf16(ob[6], ob[7])};
      const int cc = c + 32 * u;
      if (best) {
        GAS u64* hp = (GAS u64*)(ws + WS_FLAG + FL_HN + ci_ * 2048) + cc * 4;
#pragma unroll
        for (int i = 0; i < 4; ++i) __hip_atomic_store(hp + i, (1ull << 32) | (u64)pk_bf16(hn[2 * i], hn[2 * i + 1]), RLX_AGENT);
        const u32x4 b0 = {__float_as_uint(ob[0]), __float_as_uint(ob[1]), __float_as_uint(ob[2]), __float_as_uint(ob[3])};
        const u32x4 b1 = {__float_as_uint(ob[4]), __float_as_uint(ob[5]), __float_as_uint(ob[6]), __float_as_uint(ob[7])};
        __builtin_amdgcn_raw_buffer_store_b128(b0, rbase, ci_ * 2048 + cc * 32, 0, AUX_SC1);
        __builtin_amdgcn_raw_buffer_store_b128(b1, rbase, ci_ * 2048 + cc * 32 + 16, 0, AUX_SC1);
        __builtin_amdgcn_raw_buffer_store_b128(o, ro, (row * D_ + cc * 8) * 2, 0, AUX_SC1);
      } else {
        __builtin_amdgcn_raw_buffer_store_b128(o, ro, (row * D_ + cc * 8) * 2, 0, 0);
      }
    }
    WAITV(0);
    BAR();
    if (tid == 0) __hip_atomic_store((GAS unsigned*)(ws + WS_FLAG + FL_DONE) + ci_, 1u, RLX_AGENT);
    STAMP(8);
    TSDUMP(1, w8);
    return;
  }
  const int o2 = j >> 1;
  if (o2 < 7) {
    const int fp = xcd * 7 + o2;
    BAR();
    {
      const auto rg = RSRC(wg_, D_ * F_ * 2);
      const auto ru = RSRC(wu_, D_ * F_ * 2);
      const auto rw = RSRC(wd_ + (size_t)fp * 32 * D_, 32 * D_ * 2);
      const int vg = (w8 * 64 + (l >> 2)) * (F_ * 2) + fp * 64 + (l & 3) * 16;
#pragma unroll
      for (int i = 0; i < 4; ++i) {
        DMA(rg, L + (w8 * 4 + i) * 1024, vg, i * 16 * F_ * 2, 0, 0);
        DMA(ru, L + 32768 + (w8 * 4 + i) * 1024, vg, i * 16 * F_ * 2, 0, 0);
      }
#pragma unroll
      for (int i = 0; i < 4; ++i) DMA(rw, L + 65536 + (w8 * 4 + i) * 1024, l * 16, (w8 * 4 + i) * 1024, 0, 0);
    }
    STAMP(5);
    float gw[64], dw[32];
    WAITV(4);
    {
      const LAS u16* pg = (const LAS u16*)(L + h * 32768 + w8 * 4096 + r * 2);
#pragma unroll
      for (int i = 0; i < 64; ++i) gw[i] = __uint_as_float((unsigned)pg[i * 32] << 16);
#pragma unroll
      for (int i = 0; i < 64; ++i) asm volatile("" : "+v"(gw[i]));
    }
    WAITV(0);
    BAR();
    STAMP(8);
    {
      const LAS u16* pd = (const LAS u16*)(L + 65536 + tid * 2);
#pragma unroll
      for (int i = 0; i < 32; ++i) dw[i] = __uint_as_float((unsigned)pd[i * 512] << 16);
#pragma unroll
      for (int i = 0; i < 32; ++i) asm volatile("" : "+v"(dw[i]));
    }
    STAMP(9);
    LAS float* xs = (LAS float*)(L + SC_X);
    if (w8 == 4) {
      unsigned bhi, blo;
      wait_winner(ws, cand + xcd * 128, l, bhi, blo);
      STAMP(10);
      if (o2 == 0 && l < 4) __hip_atomic_store((GAS u64*)(ws + WS_FLAG + FL_WIN + (xcd * 4 + l) * 128), ((u64)bhi << 32) | (u64)blo, RLX_AGENT);
      const int wc = __builtin_amdgcn_readfirstlane(blo) & 0x7f;
      u32x4 ga, gb;
      for (unsigned spins = 0;; ++spins) {
        GAS u64* hp = (GAS u64*)(ws + WS_FLAG + FL_HN + wc * 2048) + l * 4;
        const u64 g0 = __hip_atomic_load(hp, RLX_AGENT), g1 = __hip_atomic_load(hp + 1, RLX_AGENT);
        const u64 g2 = __hip_atomic_load(hp + 2, RLX_AGENT), g3 = __hip_atomic_load(hp + 3, RLX_AGENT);
        ga = (u32x4){(unsigned)g0 << 16, (unsigned)g0 & 0xffff0000u, (unsigned)g1 << 16, (unsigned)g1 & 0xffff0000u};
        gb = (u32x4){(unsigned)g2 << 16, (unsigned)g2 & 0xffff0000u, (unsigned)g3 << 16, (unsigned)g3 & 0xffff0000u};
        const bool ok = ((g0 & g1 & g2 & g3) >> 32) != 0u;
        if (__all(ok) || spins > SPIN_CAP) { TMO(2); break; }
        asm volatile("" ::: "memory");
        __builtin_amdgcn_s_sleep(1);
      }
      *(LAS u32x4*)(xs + l * 8) = ga;
      *(LAS u32x4*)(xs + l * 8 + 4) = gb;
    }
    WAITL();
    BAR();
    STAMP(11);
    {
      float a0 = 0.f, a1 = 0.f, a2 = 0.f, a3 = 0.f;
#pragma unroll
      for (int i = 0; i < 16; ++i) {
        const f32x4 xq = *(const LAS f32x4*)(xs + w8 * 64 + i * 4);
        a0 = fmaf(xq[0], gw[4 * i], a0); a1 = fmaf(xq[1], gw[4 * i + 1], a1);
        a2 = fmaf(xq[2], gw[4 * i + 2], a2); a3 = fmaf(xq[3], gw[4 * i + 3], a3);
      }
      ((LAS float*)(L + SC_GU))[w8 * 64 + l] = (a0 + a1) + (a2 + a3);
    }
    WAITL();
    BAR();
    {
      const LAS float* gu = (const LAS float*)(L + SC_GU) + l;
      const float s = ((gu[0] + gu[64]) + (gu[128] + gu[192])) + ((gu[256] + gu[320]) + (gu[384] + gu[448]));
      auto t = __builtin_amdgcn_permlane32_swap(__float_as_uint(s), __float_as_uint(s), false, false);
      const float g = __uint_as_float(t[0]), u = __uint_as_float(t[1]);
      if (l < 32) ((LAS float*)(L + SC_ACT))[w8 * 32 + l] = g * __builtin_amdgcn_rcpf(1.0f + __expf(-g)) * u;
    }
    WAITL();
    {
      float a0 = 0.f, a1 = 0.f, a2 = 0.f, a3 = 0.f;
#pragma unroll
      for (int i = 0; i < 8; ++i) {
        const f32x4 aq = *(const LAS f32x4*)(L + SC_ACT + w8 * 128 + i * 16);
        a0 = fmaf(aq[0], dw[4 * i], a0); a1 = fmaf(aq[1], dw[4 * i + 1], a1);
        a2 = fmaf(aq[2], dw[4 * i + 2], a2); a3 = fmaf(aq[3], dw[4 * i + 3], a3);
      }
      const float y = (a0 + a1) + (a2 + a3);
      const float yn = dpp<XOR1>(y);
      if ((l & 1) == 0) {
        const int cp = tid >> 1;
        __hip_atomic_store((GAS u64*)(ws + WS_FLAG + FL_Y2 + ((cp >> 3) * 56 + fp) * 64) + (cp & 7), (1ull << 32) | (u64)pk_bf16(y, yn), RLX_AGENT);
      }
    }
    STAMP(12);
  } else if (o2 < 11 && w8 == 4) {
    const int cb = xcd * 4 + (o2 - 7);
    unsigned whi, wlo;
    for (unsigned spins = 0;; ++spins) {
      const u64 g = __hip_atomic_load((GAS u64*)(ws + WS_FLAG + FL_WIN + cb * 128), RLX_AGENT);
      whi = __builtin_amdgcn_readfirstlane((unsigned)(g >> 32)); wlo = __builtin_amdgcn_readfirstlane((unsigned)g);
      if ((wlo >> 31) != 0u || spins > SPIN_CAP) { TMO(5); break; }
      __builtin_amdgcn_s_sleep(4);
    }
    STAMP(10);
    const int wc = wlo & 0x7f, wrow = 2047 - (int)((wlo >> 8) & 0x7ffu);
    const float sig = 1.0f / (1.0f + __expf(-unord_f32(whi)));
    GAS unsigned* done = (GAS unsigned*)(ws + WS_FLAG + FL_DONE) + wc;
    for (unsigned spins = 0;; ++spins) {
      if (__builtin_amdgcn_readfirstlane(__hip_atomic_load(done, RLX_AGENT)) != 0 || spins > SPIN_CAP) { TMO(3); break; }
      __builtin_amdgcn_s_sleep(1);
    }
    const u64 bs64 = __hip_atomic_load((GAS u64*)(ws + WS_BASE + wc * 2048 + cb * 64) + (l & 7), RLX_AGENT);
    const u32x2 bs = {(unsigned)bs64, (unsigned)(bs64 >> 32)};
    STAMP(11);
    float y0, y1;
    for (unsigned spins = 0;; ++spins) {
      u64 g[7]; unsigned ok = 1u;
#pragma unroll
      for (int i = 0; i < 7; ++i) g[i] = __hip_atomic_load((GAS u64*)(ws + WS_FLAG + FL_Y2 + cb * 56 * 64) + l + i * 64, RLX_AGENT);
      y0 = 0.f; y1 = 0.f;
#pragma unroll
      for (int i = 0; i < 7; ++i) { ok &= (unsigned)(g[i] >> 32); y0 += bf_lo((unsigned)g[i]); y1 += bf_hi((unsigned)g[i]); }
      if (__all(ok != 0u) || spins > SPIN_CAP) { TMO(4); break; }
      asm volatile("" ::: "memory");
      __builtin_amdgcn_s_sleep(1);
    }
    STAMP(13);
    y0 += dpp<XOR8>(y0); y1 += dpp<XOR8>(y1);
    float yy = fold16(y0, y1);
    yy = fold32(yy, yy);
    auto t = __builtin_amdgcn_permlane16_swap(__float_as_uint(yy), __float_as_uint(yy), false, false);
    const float f0 = __uint_as_float(t[0]), f1 = __uint_as_float(t[1]);
    if (l < 8)
      __builtin_amdgcn_raw_buffer_store_b32(pk_bf16(__uint_as_float(bs[0]) + f0 * sig, __uint_as_float(bs[1]) + f1 * sig), ro,
                                            (wrow * D_ + cb * 16 + l * 2) * 2, 0, AUX_SC1);
    STAMP(14);
  }
  STAMP(15); TSDUMP(1, w8);
}

extern "C" void kernel_launch(void* const* d_in, const int* in_sizes, int n_in, void* d_out, int out_size, void* d_ws,
                              size_t ws_size, hipStream_t stream) {
  const u16* x = (const u16*)d_in[0];
  const u16* cr = (const u16*)d_in[1];
  const u16* ci = (const u16*)d_in[2];
  const u16* dr = (const u16*)d_in[3];
  const u16* di = (const u16*)d_in[4];
  const u16* wr = (const u16*)d_in[6];
  const u16* wg = (const u16*)d_in[7];
  const u16* wu = (const u16*)d_in[8];
  const u16* wd = (const u16*)d_in[9];
  unsigned char* ws = (unsigned char*)d_ws;
  u16* out = (u16*)d_out;
  (void)hipFuncSetAttribute((const void*)k1_ln_gemm, hipFuncAttributeMaxDynamicSharedMemorySize, K1_LDS);
  (void)hipFuncSetAttribute((const void*)k2_gemm, hipFuncAttributeMaxDynamicSharedMemorySize, K2_LDS);
  k1_ln_gemm<<<256, 512, K1_LDS, stream>>>(x, dr, di, cr, ci, wg, wu, wd, ws);
  k2_gemm<<<256, 512, K2_LDS, stream>>>(cr, ci, x, wr, wg, wu, wd, ws, out);
}
```

```cpp
#define K1PF 0
#define AAUX 2
#include <hip/hip_runtime.h>
#include <stdint.h>

typedef unsigned short u16;
typedef short bf16x8 __attribute__((ext_vector_type(8)));
typedef __bf16 bf16x2_t __attribute__((ext_vector_type(2)));
typedef float f32x16 __attribute__((ext_vector_type(16)));
typedef float f32x4 __attribute__((ext_vector_type(4)));
typedef float f32x2 __attribute__((ext_vector_type(2)));
typedef unsigned u32x4 __attribute__((ext_vector_type(4)));
typedef unsigned u32x2 __attribute__((ext_vector_type(2)));
typedef unsigned long long u64;
#define DI __device__ __forceinline__
#define LAS __attribute__((address_space(3)))
#define GAS __attribute__((address_space(1)))
#define MFMA32(a, b, c) __builtin_amdgcn_mfma_f32_32x32x16_bf16((a), (b), (c), 0, 0, 0)
#define MFMA16(a, b, c) __builtin_amdgcn_mfma_f32_16x16x32_bf16((a), (b), (c), 0, 0, 0)
#define DMA(rs, ldsp, voff, soff, imm, aux) __builtin_amdgcn_raw_ptr_buffer_load_lds(rs, (LAS void*)(ldsp), 16, voff, soff, imm, aux)
#define WAITV(n) asm volatile("s_waitcnt vmcnt(" #n ")" ::: "memory")
#define WAITL() asm volatile("s_waitcnt lgkmcnt(0)" ::: "memory")
#define BAR() do { asm volatile("" ::: "memory"); __builtin_amdgcn_s_barrier(); asm volatile("" ::: "memory"); } while (0)
#define SB() __builtin_amdgcn_sched_barrier(0)
#define RLX_AGENT __ATOMIC_RELAXED, __HIP_MEMORY_SCOPE_AGENT
#define RSRC(p, bytes) __builtin_amdgcn_make_buffer_rsrc((void*)(p), (short)0, (int)(bytes), 0x00020000)
constexpr int AUX_NT = 2, AUX_SC1 = 16;
#define K1AUX 2
#define K2TOUCH 1

constexpr int S_ = 2048, D_ = 512, F_ = 1792;
#define TSDECL
#define STAMP(i)
#define TSDUMP(kern, wave)

DI float bf_lo(unsigned w) { return __uint_as_float(w << 16); }
DI float bf_hi(unsigned w) { return __uint_as_float(w & 0xffff0000u); }
DI unsigned pk_bf16(float lo, float hi) {
  f32x2 v = {lo, hi};
  bf16x2_t b = __builtin_convertvector(v, bf16x2_t);
  return __builtin_bit_cast(unsigned, b);
}
DI float dot2(unsigned a, unsigned b, float acc) {
  return __builtin_amdgcn_fdot2_f32_bf16(__builtin_bit_cast(bf16x2_t, a), __builtin_bit_cast(bf16x2_t, b), acc, false);
}
template <int CTRL> DI float dpp(float x) {
  return __builtin_bit_cast(float, __builtin_amdgcn_mov_dpp(__builtin_bit_cast(int, x), CTRL, 0xf, 0xf, true));
}
template <int CTRL> DI unsigned dppu(unsigned x) { return (unsigned)__builtin_amdgcn_mov_dpp((int)x, CTRL, 0xf, 0xf, true); }
constexpr int XOR1 = 0xB1, XOR2 = 0x4E, XOR7 = 0x141, XOR8 = 0x128;
DI float fold32(float a, float b) {
  auto t = __builtin_amdgcn_permlane32_swap(__float_as_uint(a), __float_as_uint(b), false, false);
  return __uint_as_float(t[0]) + __uint_as_float(t[1]);
}
DI float fold16(float a, float b) {
  auto t = __builtin_amdgcn_permlane16_swap(__float_as_uint(a), __float_as_uint(b), false, false);
  return __uint_as_float(t[0]) + __uint_as_float(t[1]);
}
DI float bsel(unsigned m, float a, float b) { return __uint_as_float((m & __float_as_uint(a)) | (~m & __float_as_uint(b))); }
DI unsigned ord_f32(float v) { const unsigned u = __float_as_uint(v); return u ^ ((unsigned)((int)u >> 31) | 0x80000000u); }
DI float unord_f32(unsigned o) { return __uint_as_float(o ^ ((o >> 31) ? 0x80000000u : 0xffffffffu)); }
DI void max2(unsigned& hi, unsigned& lo, unsigned ohi, unsigned olo) {
  const bool t = (ohi > hi) | ((ohi == hi) & (olo > lo));
  hi = t ? ohi : hi; lo = t ? olo : lo;
}

constexpr size_t WS_AB = 0;
constexpr size_t WS_PART = WS_AB + (size_t)4096 * 512 * 2;
constexpr size_t WS_BASE = WS_PART + (size_t)256 * 32768;
constexpr size_t WS_FLAG = WS_BASE + (size_t)128 * 2048;
constexpr int FL_HN = 0;
constexpr int FL_Y2 = 128 * 2048;
constexpr int FL_ARR = FL_Y2 + 32 * 56 * 64;
constexpr int FL_CAND = FL_ARR + 128 * 128;
constexpr int FL_WIN = FL_CAND + 8 * 1024;
constexpr int FL_DONE = FL_WIN + 32 * 128;
constexpr int FL_PER = 1664;
constexpr int FL_BYTES = 256 * FL_PER;
static_assert(FL_DONE + 512 <= FL_BYTES, "flag area");
constexpr unsigned SPIN_CAP = 1u << 18;
#define TMO(code)

#define ABAUX 0
#define WAUX 0
#define WPF 0
constexpr int K1_BUF = 24576, K1_ST = 6 * K1_BUF, K1_SD = K1_ST + 2048, K1_PF = K1_SD + 256, K1_LDS = K1_PF + 2048;
__global__ __launch_bounds__(512, 2) void k1_ln_gemm(const u16* __restrict__ x, const u16* __restrict__ dr,
                                                     const u16* __restrict__ di, const u16* __restrict__ cr,
                                                     const u16* __restrict__ ci, const u16* __restrict__ wg_,
                                                     const u16* __restrict__ wu_, const u16* __restrict__ wd_,
                                                     unsigned char* ws) {
  extern __shared__ __attribute__((aligned(16))) unsigned char smem[];
  LAS unsigned char* L = (LAS unsigned char*)smem;
  const int tid = threadIdx.x, w8 = __builtin_amdgcn_readfirstlane(tid >> 6), w = w8 & 3, l = tid & 63, r = l & 31, h = l >> 5;
  const int b = blockIdx.x, xcd = b & 7, j = b >> 3;
  TSDECL STAMP(0);
  const int isB = xcd >> 2, sb = ((xcd >> 1) & 1) * 8 + (xcd & 1) * 4 + (j & 3), e8 = j >> 2;
  const u16* dm = (isB ? di : dr) + (size_t)e8 * 64 * D_;
  const auto rx = RSRC(x + (size_t)sb * 128 * D_, 128 * 1024);
  const auto rd = RSRC(dm, 64 * 1024);
  const int v0 = (l >> 3) * 1024 + (((l & 7) ^ ((l >> 4) & 7)) * 16);
  const int v1 = (l >> 3) * 1024 + (((l & 7) ^ ((4 + (l >> 4)) & 7)) * 16) + 7 * 1024;
  const int rot = (e8 + 2 * (j & 3)) & 7;
  auto issue = [&](int t_, int buf) {
    const int ko = ((t_ + rot) & 7) * 128;
    LAS unsigned char* bx = L + buf * K1_BUF + w * 4096;
    LAS unsigned char* bd = L + buf * K1_BUF + 16384 + w * 2048;
    DMA(rd, bd, v0, 16 * w * 1024 + ko, 0, K1AUX);
    DMA(rd, bd, v1, 16 * w * 1024 + ko, 1024, K1AUX);
    DMA(rx, bx, v0, 32 * w * 1024 + ko, 0, K1AUX);
    DMA(rx, bx, v1, 32 * w * 1024 + ko, 1024, K1AUX);
    DMA(rx, bx, v0, 32 * w * 1024 + ko + 14 * 1024, 2048, K1AUX);
    DMA(rx, bx, v1, 32 * w * 1024 + ko + 14 * 1024, 3072, K1AUX);
  };
  issue(w8 >> 2, w8 >> 2);
  STAMP(1);
  const int i16 = l & 15, q4 = l >> 4;
  if (w8 >= 4) {
    issue(2, 2);
    f32x4 sdv = {};
    const bf16x8 ones = {0x3f80, 0x3f80, 0x3f80, 0x3f80, 0x3f80, 0x3f80, 0x3f80, 0x3f80};
    const int sdd = 16384 + (w * 16 + i16) * 128;
    const int ps0 = (q4 ^ (i16 >> 1)) * 16, ps1 = ((4 + q4) ^ (i16 >> 1)) * 16;
#pragma unroll
    for (int t = 0; t < 8; ++t) {
      if (t >= 1 && t < 5) WAITV(12); else if (t == 5) WAITV(15); else if (t == 6) WAITV(9); else if (t == 7) WAITV(3);
      if (t == 1) STAMP(12); if (t == 2) STAMP(13); if (t == 6) STAMP(14); if (t == 7) STAMP(15);
      WAITL();
      BAR();
      const LAS unsigned char* bb = L + (t % 6) * K1_BUF;
      const bf16x8 d0 = *(const LAS bf16x8*)(bb + sdd + ps0), d1 = *(const LAS bf16x8*)(bb + sdd + ps1);
      SB();
      if (t < 5) issue(t + 3, (t + 3) % 6);
      if (t == 4) {
        const int ks = xcd >> 1;
        const u16* A = ((ks >> 1) ? ci : cr) + (size_t)((xcd & 1) * 1024 + j * 32) * S_ + (ks & 1) * 1024;
        const auto rp = RSRC(A, K1PF ? 31 * 4096 + 2048 : 0);
        const int vo = (w * 8 + (l >> 4)) * 4096 + (l & 15) * 128;
        __builtin_amdgcn_raw_ptr_buffer_load_lds(rp, (LAS void*)(L + K1_PF + w * 512), 4, vo, 0, 0, AUX_NT);
        __builtin_amdgcn_raw_ptr_buffer_load_lds(rp, (LAS void*)(L + K1_PF + w * 512 + 256), 4, vo, 4 * 4096, 0, AUX_NT);
        const u16* wp = w == 0 ? wg_ : w == 1 ? wu_ : wd_ + (size_t)(xcd * 224 + j * 7) * D_;
        const auto rq = RSRC(wp, !WPF ? 0 : w < 2 ? D_ * F_ * 2 : w == 2 ? 7 * D_ * 2 : 0);
        const int vw = w < 2 ? (j * 16 + (l >> 2)) * (F_ * 2) + ((xcd * 448) & ~127) + (l & 3) * 128 : l * 128;
        __builtin_amdgcn_raw_ptr_buffer_load_lds(rq, (LAS void*)(L + K1_PF + w * 512), 4, vw, 0, 0, WAUX);
        const int t4 = tid - 256;
        if (t4 < FL_PER / 16) *(u32x4*)(ws + WS_FLAG + (size_t)b * FL_PER + t4 * 16) = (u32x4){0u, 0u, 0u, 0u};
      }
      SB();
      sdv = MFMA16(d0, ones, sdv);
      sdv = MFMA16(d1, ones, sdv);
      SB();
    }
    if (i16 == 0) *(LAS f32x4*)(L + K1_SD + (w * 16 + q4 * 4) * 4) = sdv;
    WAITL();
    BAR();
    STAMP(6);
    STAMP(8); TSDUMP(0, w8);
    return;
  }
  f32x16 acc0 = {}, acc1 = {}, gram = {};
  f32x4 rsum = {};
  const short sv = (i16 == (q4 & 1)) ? (short)0x3f80 : (short)0;
  const bf16x8 selB = {sv, sv, sv, sv, sv, sv, sv, sv};
  bf16x8 fx[2][4], f0[2][4], f1[2][4];
  int pos[4];
#pragma unroll
  for (int kk = 0; kk < 4; ++kk) pos[kk] = r * 128 + (((2 * kk + h) ^ ((r >> 1) & 7)) * 16);
  auto ldf = [&](int t, int kk) {
    const LAS unsigned char* bb = L + (t % 6) * K1_BUF;
    fx[t & 1][kk] = *(const LAS bf16x8*)(bb + w * 4096 + pos[kk]);
    f0[t & 1][kk] = *(const LAS bf16x8*)(bb + 16384 + pos[kk]);
    f1[t & 1][kk] = *(const LAS bf16x8*)(bb + 16384 + 4096 + pos[kk]);
  };
  WAITV(0);
  BAR();
  STAMP(2);
#pragma unroll
  for (int kk = 0; kk < 4; ++kk) ldf(0, kk);
#pragma unroll
  for (int t = 0; t < 7; ++t) {
    const int p = t & 1;
    WAITL();
    BAR();
    if (t == 3) STAMP(3);
#pragma unroll
    for (int kk = 0; kk < 4; ++kk) {
      ldf(t + 1, kk);
      SB();
      gram = MFMA32(fx[p][kk], fx[p][kk], gram);
      rsum = MFMA16(fx[p][kk], selB, rsum);
      acc0 = MFMA32(fx[p][kk], f0[p][kk], acc0);
      acc1 = MFMA32(fx[p][kk], f1[p][kk], acc1);
      SB();
    }
  }
#pragma unroll
  for (int kk = 0; kk < 4; ++kk) { gram = MFMA32(fx[1][kk], fx[1][kk], gram); rsum = MFMA16(fx[1][kk], selB, rsum); }
  SB();
  acc0 = MFMA32(fx[1][0], f0[1][0], acc0); acc1 = MFMA32(fx[1][0], f1[1][0], acc1);
  acc0 = MFMA32(fx[1][1], f0[1][1], acc0); acc1 = MFMA32(fx[1][1], f1[1][1], acc1);
  SB();
  LAS float* st = (LAS float*)(L + K1_ST + w * 512);
  if (i16 < 2) *(LAS f32x4*)(st + i16 * 16 + q4 * 4) = rsum;
  float dg;
  {
    const unsigned m0 = 0u - (unsigned)(r & 1), m1 = 0u - (unsigned)((r >> 1) & 1), m3 = 0u - (unsigned)((r >> 3) & 1), m4 = 0u - (unsigned)(r >> 4);
    float e[4];
#pragma unroll
    for (int g = 0; g < 4; ++g) e[g] = bsel(m1, bsel(m0, gram[4 * g + 3], gram[4 * g + 2]), bsel(m0, gram[4 * g + 1], gram[4 * g]));
    dg = bsel(m4, bsel(m3, e[3], e[2]), bsel(m3, e[1], e[0]));
  }
  SB();
  acc0 = MFMA32(fx[1][2], f0[1][2], acc0); acc1 = MFMA32(fx[1][2], f1[1][2], acc1);
  SB();
  {
    const float mean = st[r] * (1.0f / D_);
    float rs = __builtin_amdgcn_rsqf(fmaxf(dg * (1.0f / D_) - mean * mean, 0.f) + 1e-5f);
    if (isB) rs = -rs;
    if (h == ((r >> 2) & 1)) *(LAS f32x2*)(st + 32 + 2 * r) = (f32x2){mean * rs, rs};
  }
  SB();
  acc0 = MFMA32(fx[1][3], f0[1][3], acc0); acc1 = MFMA32(fx[1][3], f1[1][3], acc1);
  SB();
  STAMP(6);
  WAITL();
  BAR();
  const LAS float* sdl = (const LAS float*)(L + K1_SD);
  const float cs0 = sdl[r], cs1 = sdl[32 + r];
  const LAS f32x4* st4 = (const LAS f32x4*)(st + 32);
  unsigned p0[8], p1[8];
#pragma unroll
  for (int g = 0; g < 4; ++g) {
    const f32x4 sa = st4[4 * g + 2 * h], sbv = st4[4 * g + 2 * h + 1];
    p0[2 * g] = pk_bf16(acc0[4 * g] * sa[1] - sa[0] * cs0, acc0[4 * g + 1] * sa[3] - sa[2] * cs0);
    p0[2 * g + 1] = pk_bf16(acc0[4 * g + 2] * sbv[1] - sbv[0] * cs0, acc0[4 * g + 3] * sbv[3] - sbv[2] * cs0);
    p1[2 * g] = pk_bf16(acc1[4 * g] * sa[1] - sa[0] * cs1, acc1[4 * g + 1] * sa[3] - sa[2] * cs1);
    p1[2 * g + 1] = pk_bf16(acc1[4 * g + 2] * sbv[1] - sbv[0] * cs1, acc1[4 * g + 3] * sbv[3] - sbv[2] * cs1);
  }
  const int kk0 = isB * 2048 + sb * 128 + w * 32;
  const int kb = kk0 >> 6, kst = (kk0 >> 4) & 3;
  const int n0 = e8 * 64, nb = n0 >> 7, nt0 = (n0 >> 5) & 3;
  const auto rab = RSRC(ws + WS_AB + (size_t)(nb * 64 + kb) * 16384, 16384);
#pragma unroll
  for (int s2 = 0; s2 < 2; ++s2) {
    {
      auto t0 = __builtin_amdgcn_permlane32_swap(p0[4 * s2], p0[4 * s2 + 2], false, false);
      auto t1 = __builtin_amdgcn_permlane32_swap(p0[4 * s2 + 1], p0[4 * s2 + 3], false, false);
      u32x4 o = {t0[0], t1[0], t0[1], t1[1]};
      __builtin_amdgcn_raw_buffer_store_b128(o, rab, l * 16, ((kst + s2) * 4 + nt0) * 1024, ABAUX);
    }
    {
      auto t0 = __builtin_amdgcn_permlane32_swap(p1[4 * s2], p1[4 * s2 + 2], false, false);
      auto t1 = __builtin_amdgcn_permlane32_swap(p1[4 * s2 + 1], p1[4 * s2 + 3], false, false);
      u32x4 o = {t0[0], t1[0], t0[1], t1[1]};
      __builtin_amdgcn_raw_buffer_store_b128(o, rab, l * 16, ((kst + s2) * 4 + nt0 + 1) * 1024, ABAUX);
    }
  }
  STAMP(7);
  STAMP(8); TSDUMP(0, w8);
}

constexpr int SC_RED = 4 * 32768;
constexpr int SC_X = SC_RED + 2048;
constexpr int SC_GU = SC_X + 2048;
constexpr int SC_ACT = SC_GU + 2048;
constexpr int K2_LDS = SC_ACT + 1024;

DI void wait_winner(unsigned char* ws, GAS u64* cand, int l, unsigned& bhi, unsigned& blo) {
  for (unsigned spins = 0;; ++spins) {
    const u64 g0 = __hip_atomic_load(cand + l, RLX_AGENT), g1 = __hip_atomic_load(cand + 64 + l, RLX_AGENT);
    bhi = (unsigned)(g0 >> 32); blo = (unsigned)g0;
    max2(bhi, blo, (unsigned)(g1 >> 32), (unsigned)g1);
    if (__all((((unsigned)g0 & (unsigned)g1) >> 31) != 0u) || spins > SPIN_CAP) { TMO(1); break; }
    __builtin_amdgcn_s_sleep(2);
  }
  max2(bhi, blo, dppu<XOR1>(bhi), dppu<XOR1>(blo));
  max2(bhi, blo, dppu<XOR2>(bhi), dppu<XOR2>(blo));
  max2(bhi, blo, dppu<XOR7>(bhi), dppu<XOR7>(blo));
  max2(bhi, blo, dppu<XOR8>(bhi), dppu<XOR8>(blo));
  {
    auto th = __builtin_amdgcn_permlane16_swap(bhi, bhi, false, false);
    auto tl = __builtin_amdgcn_permlane16_swap(blo, blo, false, false);
    bhi = th[0]; blo = tl[0]; max2(bhi, blo, th[1], tl[1]);
  }
  {
    auto th = __builtin_amdgcn_permlane32_swap(bhi, bhi, false, false);
    auto tl = __builtin_amdgcn_permlane32_swap(blo, blo, false, false);
    bhi = th[0]; blo = tl[0]; max2(bhi, blo, th[1], tl[1]);
  }
}

__global__ __launch_bounds__(512, 2) void k2_gemm(const u16* __restrict__ cr, const u16* __restrict__ ci,
                                                  const u16* __restrict__ x, const u16* __restrict__ wr,
                                                  const u16* __restrict__ wg_, const u16* __restrict__ wu_,
                                                  const u16* __restrict__ wd_, unsigned char* ws,
                                                  u16* out) {
  extern __shared__ __attribute__((aligned(16))) unsigned char smem[];
  LAS unsigned char* L = (LAS unsigned char*)smem;
  const int tid = threadIdx.x, w8 = __builtin_amdgcn_readfirstlane(tid >> 6), w = w8 & 3, l = tid & 63, r = l & 31, h = l >> 5;
  const int b = blockIdx.x, xcd = b & 7, j = b >> 3;
  const int ks = xcd >> 1, mb = (xcd & 1) * 8 + (j >> 2), nb = j & 3;
  TSDECL STAMP(0);
  const u16* A = ((ks >> 1) ? ci : cr) + (size_t)mb * 128 * S_ + (ks & 1) * 1024;
  const auto ra = RSRC(A, 127 * 4096 + 2048);
  const auto rb = RSRC(ws + WS_AB + (size_t)(nb * 64 + ks * 16) * 16384, 16 * 16384);
  const int va0 = (32 * w + (l >> 3)) * 4096 + (((l & 7) ^ ((l >> 4) & 7)) * 16);
  const int va1 = (32 * w + (l >> 3)) * 4096 + (((l & 7) ^ ((4 + (l >> 4)) & 7)) * 16);
  const int vb = w * 4096 + l * 16;
  auto stage = [&](int t, int buf) {
    DMA(ra, L + buf * 32768 + w * 4096, va0, t * 128, 0, AAUX);
    DMA(ra, L + buf * 32768 + w * 4096, va1, t * 128 + 1 * (32768 - 1024), 1024, AAUX);
    DMA(ra, L + buf * 32768 + w * 4096, va0, t * 128 + 2 * (32768 - 1024), 2048, AAUX);
    DMA(ra, L + buf * 32768 + w * 4096, va1, t * 128 + 3 * (32768 - 1024), 3072, AAUX);
    DMA(rb, L + buf * 32768 + 16384 + w * 4096, vb, t * 16384, 0, 0);
    DMA(rb, L + buf * 32768 + 16384 + w * 4096, vb, t * 16384, 1024, 0);
    DMA(rb, L + buf * 32768 + 16384 + w * 4096, vb, t * 16384, 2048, 0);
    DMA(rb, L + buf * 32768 + 16384 + w * 4096, vb, t * 16384, 3072, 0);
  };
  stage(w8 >> 2, w8 >> 2);
  if (w8 >= 4) {
    BAR();
    stage(2, 2); stage(3, 3);
#pragma unroll
    for (int t = 0; t < 12; ++t) {
      WAITV(16);
      BAR();
      stage(t + 4, t & 3);
    }
    WAITV(16); BAR(); WAITV(8); BAR(); WAITV(0); BAR();
    STAMP(2);
  } else {
    const int wm = w >> 1, wn = w & 1;
    f32x16 acc[2][2] = {};
    int aoff[4];
    const int sw = (r >> 1) & 7;
#pragma unroll
    for (int kk = 0; kk < 4; ++kk) aoff[kk] = (wm * 64 + r) * 128 + (((2 * kk + h) ^ sw) * 16);
    const int boff = 16384 + wn * 2048 + l * 16;
    bf16x8 fa[2][2], fb[2][2];
    auto ldf = [&](int t, int kk, int p) {
      const int bo = (t & 3) * 32768;
      fb[p][0] = *(const LAS bf16x8*)(L + bo + boff + kk * 4096);
      fa[p][0] = *(const LAS bf16x8*)(L + bo + aoff[kk]);
      fa[p][1] = *(const LAS bf16x8*)(L + bo + aoff[kk] + 4096);
      fb[p][1] = *(const LAS bf16x8*)(L + bo + boff + kk * 4096 + 1024);
    };
    auto mma = [&](int p) {
      acc[0][0] = MFMA32(fb[p][0], fa[p][0], acc[0][0]);
      acc[1][0] = MFMA32(fb[p][0], fa[p][1], acc[1][0]);
      acc[0][1] = MFMA32(fb[p][1], fa[p][0], acc[0][1]);
      acc[1][1] = MFMA32(fb[p][1], fa[p][1], acc[1][1]);
      SB();
    };
    WAITV(0);
    BAR();
    STAMP(1);
    ldf(0, 0, 0);
#pragma unroll
    for (int t = 0; t < 16; ++t) {
      ldf(t, 1, 1); SB(); mma(0);
      ldf(t, 2, 0); SB(); mma(1);
      ldf(t, 3, 1); SB(); mma(0);
      if (t < 15) { WAITL(); BAR(); ldf(t + 1, 0, 0); SB(); }
      mma(1);
    }
    STAMP(2);
    const auto rp = RSRC(ws + WS_PART + (size_t)((ks * 16 + mb) * 4 + nb) * 32768, 32768);
#pragma unroll
    for (int nt = 0; nt < 2; ++nt)
#pragma unroll
      for (int mt = 0; mt < 2; ++mt)
#pragma unroll
        for (int s2 = 0; s2 < 2; ++s2) {
          const f32x16& a = acc[mt][nt];
          const unsigned p0 = pk_bf16(a[8 * s2], a[8 * s2 + 1]), p1 = pk_bf16(a[8 * s2 + 2], a[8 * s2 + 3]);
          const unsigned p2 = pk_bf16(a[8 * s2 + 4], a[8 * s2 + 5]), p3 = pk_bf16(a[8 * s2 + 6], a[8 * s2 + 7]);
          auto t0 = __builtin_amdgcn_permlane32_swap(p0, p2, false, false);
          auto t1 = __builtin_amdgcn_permlane32_swap(p1, p3, false, false);
          const u32x4 o = {t0[0], t1[0], t0[1], t1[1]};
          __builtin_amdgcn_raw_buffer_store_b128(o, rp, (((w * 2 + mt) * 2 + nt) * 2 + s2) * 1024 + l * 16, 0, AUX_SC1);
          SB();
        }
    STAMP(3);
    WAITV(0);
    if (l < 4)
      __hip_atomic_store((GAS unsigned*)(ws + WS_FLAG + FL_ARR) + (mb * 8 + wm * 4 + l) * 32 + (ks * 4 + nb) * 2 + wn, 1u, RLX_AGENT);
    STAMP(4);
  }
  GAS u64* cand = (GAS u64*)(ws + WS_FLAG + FL_CAND);
  const auto ro = RSRC(out, S_ * D_ * 2);
  const auto rbase = RSRC(ws + WS_BASE, 128 * 2048);

  if ((j & 1) == 0) {
    const int q2 = ks * 2 + (nb >> 1), ci_ = mb * 8 + q2;
    const int r16 = l & 15, c = tid >> 4;
    const int row = mb * 128 + q2 * 16 + r16;
    BAR();
    u32x4 xv[2], wv[2];
#pragma unroll
    for (int u = 0; u < 2; ++u) {
      xv[u] = __builtin_nontemporal_load((const u32x4*)(x + (size_t)row * D_ + c * 8 + u * 256));
      wv[u] = *(const u32x4*)(wr + c * 8 + u * 256);
    }
    if (w8 == 5) {
      GAS unsigned* arr = (GAS unsigned*)(ws + WS_FLAG + FL_ARR) + ci_ * 32 + r;
      for (unsigned spins = 0;; ++spins) {
        if (__all(__hip_atomic_load(arr, RLX_AGENT) != 0u) || spins > SPIN_CAP) { TMO(0); break; }
        __builtin_amdgcn_s_sleep(1);
      }
    }
    const auto rp = RSRC(ws + WS_PART, 256 * 32768);
    const int poff = (mb * 4 + (c >> 4)) * 32768 +
                     (((((q2 >> 2) * 2 + ((c >> 3) & 1)) * 2 + ((q2 >> 1) & 1)) * 2 + ((c >> 2) & 1)) * 2 + ((c >> 1) & 1)) * 1024 +
                     ((c & 1) * 32 + (q2 & 1) * 16 + r16) * 16;
    SB();
    BAR();
    STAMP(5);
    u32x4 pv[2][4];
#pragma unroll
    for (int u = 0; u < 2; ++u)
#pragma unroll
      for (int k2 = 0; k2 < 4; ++k2) pv[u][k2] = __builtin_amdgcn_raw_buffer_load_b128(rp, poff, (k2 * 64 + u * 2) * 32768, AUX_SC1);
    float sw = 0.f, wf[16], hv[16];
#pragma unroll
    for (int u = 0; u < 2; ++u)
#pragma unroll
      for (int i = 0; i < 4; ++i) {
        wf[8 * u + 2 * i] = bf_lo(wv[u][i]); wf[8 * u + 2 * i + 1] = bf_hi(wv[u][i]);
        sw += wf[8 * u + 2 * i] + wf[8 * u + 2 * i + 1];
        hv[8 * u + 2 * i] = bf_lo(xv[u][i]) + ((bf_lo(pv[u][0][i]) + bf_lo(pv[u][1][i])) + (bf_lo(pv[u][2][i]) + bf_lo(pv[u][3][i])));
        hv[8 * u + 2 * i + 1] = bf_hi(xv[u][i]) + ((bf_hi(pv[u][0][i]) + bf_hi(pv[u][1][i])) + (bf_hi(pv[u][2][i]) + bf_hi(pv[u][3][i])));
      }
    float mu = 0.f, m2 = 0.f, hw = 0.f;
#pragma unroll
    for (int i = 0; i < 4; ++i) mu += (hv[4 * i] + hv[4 * i + 1]) + (hv[4 * i + 2] + hv[4 * i + 3]);
    mu *= 0.0625f;
    STAMP(6);
#pragma unroll
    for (int i = 0; i < 16; ++i) { const float d = hv[i] - mu; m2 = fmaf(d, d, m2); hw = fmaf(hv[i], wf[i], hw); }
    float hs = fold16(hw, sw);
    {
      auto t = __builtin_amdgcn_permlane16_swap(__float_as_uint(mu), __float_as_uint(mu), false, false);
      const float a = __uint_as_float(t[0]), bb = __uint_as_float(t[1]), d = a - bb;
      m2 = fold16(m2, m2) + d * d * 8.0f; mu = (a + bb) * 0.5f;
    }
    hs = fold32(hs, hs);
    {
      auto t = __builtin_amdgcn_permlane32_swap(__float_as_uint(mu), __float_as_uint(mu), false, false);
      const float a = __uint_as_float(t[0]), bb = __uint_as_float(t[1]), d = a - bb;
      m2 = fold32(m2, m2) + d * d * 16.0f; mu = (a + bb) * 0.5f;
    }
    LAS float* red = (LAS float*)(L + SC_RED);
    if (l < 16) *(LAS f32x2*)(red + (l * 8 + w8) * 4) = (f32x2){mu, m2};
    if (l < 32) red[(r16 * 8 + w8) * 4 + 2 + (l >> 4)] = hs;
    WAITL();
    BAR();
    float mean = 0.f, M2 = 0.f, HW = 0.f, SW = 0.f;
    {
      f32x4 t[8];
#pragma unroll
      for (int i = 0; i < 8; ++i) t[i] = *(const LAS f32x4*)(red + (r16 * 8 + i) * 4);
#pragma unroll
      for (int i = 0; i < 8; ++i) { mean += t[i][0]; M2 += t[i][1]; HW += t[i][2]; SW += t[i][3]; }
      mean *= 0.125f;
#pragma unroll
      for (int i = 0; i < 8; ++i) { const float d = t[i][0] - mean; M2 = fmaf(d * d, 64.0f, M2); }
    }
    const float rstd = __builtin_amdgcn_rsqf(M2 * (1.0f / D_) + 1e-5f);
    const float rr = (HW - mean * SW) * rstd;
    unsigned khi = ord_f32(rr), klo = 0x80000000u | (unsigned)((2047 - row) << 8) | (unsigned)ci_;
    max2(khi, klo, dppu<XOR1>(khi), dppu<XOR1>(klo));
    max2(khi, klo, dppu<XOR2>(khi), dppu<XOR2>(klo));
    max2(khi, klo, dppu<XOR7>(khi), dppu<XOR7>(klo));
    max2(khi, klo, dppu<XOR8>(khi), dppu<XOR8>(klo));
    if (tid < 8) __hip_atomic_store(cand + tid * 128 + ci_, ((u64)khi << 32) | (u64)klo, RLX_AGENT);
    STAMP(7);
    const bool best = ((2047 - (int)((klo >> 8) & 0x7ffu)) == row);
#pragma unroll
    for (int u = 0; u < 2; ++u) {
      float hn[8], ob[8];
#pragma unroll
      for (int i = 0; i < 8; ++i) { hn[i] = (hv[8 * u + i] - mean) * rstd; ob[i] = hv[8 * u + i] + hn[i]; }
      const u32x4 o = {pk_bf16(ob[0], ob[1]), pk_bf16(ob[2], ob[3]), pk_bf16(ob[4], ob[5]), pk_bf16(ob[6], ob[7])};
      const int cc = c + 32 * u;
      if (best) {
        GAS u64* hp = (GAS u64*)(ws + WS_FLAG + FL_HN + ci_ * 2048) + cc * 4;
#pragma unroll
        for (int i = 0; i < 4; ++i) __hip_atomic_store(hp + i, (1ull << 32) | (u64)pk_bf16(hn[2 * i], hn[2 * i + 1]), RLX_AGENT);
        const u32x4 b0 = {__float_as_uint(ob[0]), __float_as_uint(ob[1]), __float_as_uint(ob[2]), __float_as_uint(ob[3])};
        const u32x4 b1 = {__float_as_uint(ob[4]), __float_as_uint(ob[5]), __float_as_uint(ob[6]), __float_as_uint(ob[7])};
        __builtin_amdgcn_raw_buffer_store_b128(b0, rbase, ci_ * 2048 + cc * 32, 0, AUX_SC1);
        __builtin_amdgcn_raw_buffer_store_b128(b1, rbase, ci_ * 2048 + cc * 32 + 16, 0, AUX_SC1);
        __builtin_amdgcn_raw_buffer_store_b128(o, ro, (row * D_ + cc * 8) * 2, 0, AUX_SC1);
      } else {
        __builtin_amdgcn_raw_buffer_store_b128(o, ro, (row * D_ + cc * 8) * 2, 0, 0);
      }
    }
    WAITV(0);
    BAR();
    if (tid == 0) __hip_atomic_store((GAS unsigned*)(ws + WS_FLAG + FL_DONE) + ci_, 1u, RLX_AGENT);
    STAMP(8);
    TSDUMP(1, w8);
    return;
  }
  const int o2 = j >> 1;
  if (o2 < 7) {
    const int fp = xcd * 7 + o2;
    BAR();
    {
      const auto rg = RSRC(wg_, D_ * F_ * 2);
      const auto ru = RSRC(wu_, D_ * F_ * 2);
      const auto rw = RSRC(wd_ + (size_t)fp * 32 * D_, 32 * D_ * 2);
      const int vg = (w8 * 64 + (l >> 2)) * (F_ * 2) + fp * 64 + (l & 3) * 16;
#pragma unroll
      for (int i = 0; i < 4; ++i) {
        DMA(rg, L + (w8 * 4 + i) * 1024, vg, i * 16 * F_ * 2, 0, 0);
        DMA(ru, L + 32768 + (w8 * 4 + i) * 1024, vg, i * 16 * F_ * 2, 0, 0);
      }
#pragma unroll
      for (int i = 0; i < 4; ++i) DMA(rw, L + 65536 + (w8 * 4 + i) * 1024, l * 16, (w8 * 4 + i) * 1024, 0, 0);
    }
    STAMP(5);
    float gw[64], dw[32];
    WAITV(4);
    {
      const LAS u16* pg = (const LAS u16*)(L + h * 32768 + w8 * 4096 + r * 2);
#pragma unroll
      for (int i = 0; i < 64; ++i) gw[i] = __uint_as_float((unsigned)pg[i * 32] << 16);
#pragma unroll
      for (int i = 0; i < 64; ++i) asm volatile("" : "+v"(gw[i]));
    }
    WAITV(0);
    BAR();
    STAMP(8);
    {
      const LAS u16* pd = (const LAS u16*)(L + 65536 + tid * 2);
#pragma unroll
      for (int i = 0; i < 32; ++i) dw[i] = __uint_as_float((unsigned)pd[i * 512] << 16);
#pragma unroll
      for (int i = 0; i < 32; ++i) asm volatile("" : "+v"(dw[i]));
    }
    STAMP(9);
    LAS float* xs = (LAS float*)(L + SC_X);
    if (w8 == 4) {
      unsigned bhi, blo;
      wait_winner(ws, cand + xcd * 128, l, bhi, blo);
      STAMP(10);
      if (o2 == 0 && l < 4) __hip_atomic_store((GAS u64*)(ws + WS_FLAG + FL_WIN + (xcd * 4 + l) * 128), ((u64)bhi << 32) | (u64)blo, RLX_AGENT);
      const int wc = __builtin_amdgcn_readfirstlane(blo) & 0x7f;
      u32x4 ga, gb;
      for (unsigned spins = 0;; ++spins) {
        GAS u64* hp = (GAS u64*)(ws + WS_FLAG + FL_HN + wc * 2048) + l * 4;
        const u64 g0 = __hip_atomic_load(hp, RLX_AGENT), g1 = __hip_atomic_load(hp + 1, RLX_AGENT);
        const u64 g2 = __hip_atomic_load(hp + 2, RLX_AGENT), g3 = __hip_atomic_load(hp + 3, RLX_AGENT);
        ga = (u32x4){(unsigned)g0 << 16, (unsigned)g0 & 0xffff0000u, (unsigned)g1 << 16, (unsigned)g1 & 0xffff0000u};
        gb = (u32x4){(unsigned)g2 << 16, (unsigned)g2 & 0xffff0000u, (unsigned)g3 << 16, (unsigned)g3 & 0xffff0000u};
        const bool ok = ((g0 & g1 & g2 & g3) >> 32) != 0u;
        if (__all(ok) || spins > SPIN_CAP) { TMO(2); break; }
        asm volatile("" ::: "memory");
        __builtin_amdgcn_s_sleep(1);
      }
      *(LAS u32x4*)(xs + l * 8) = ga;
      *(LAS u32x4*)(xs + l * 8 + 4) = gb;
    }
    WAITL();
    BAR();
    STAMP(11);
    {
      float a0 = 0.f, a1 = 0.f, a2 = 0.f, a3 = 0.f;
#pragma unroll
      for (int i = 0; i < 16; ++i) {
        const f32x4 xq = *(const LAS f32x4*)(xs + w8 * 64 + i * 4);
        a0 = fmaf(xq[0], gw[4 * i], a0); a1 = fmaf(xq[1], gw[4 * i + 1], a1);
        a2 = fmaf(xq[2], gw[4 * i + 2], a2); a3 = fmaf(xq[3], gw[4 * i + 3], a3);
      }
      ((LAS float*)(L + SC_GU))[w8 * 64 + l] = (a0 + a1) + (a2 + a3);
    }
    WAITL();
    BAR();
    {
      const LAS float* gu = (const LAS float*)(L + SC_GU) + l;
      const float s = ((gu[0] + gu[64]) + (gu[128] + gu[192])) + ((gu[256] + gu[320]) + (gu[384] + gu[448]));
      auto t = __builtin_amdgcn_permlane32_swap(__float_as_uint(s), __float_as_uint(s), false, false);
      const float g = __uint_as_float(t[0]), u = __uint_as_float(t[1]);
      if (l < 32) ((LAS float*)(L + SC_ACT))[w8 * 32 + l] = g * __builtin_amdgcn_rcpf(1.0f + __expf(-g)) * u;
    }
    WAITL();
    {
      float a0 = 0.f, a1 = 0.f, a2 = 0.f, a3 = 0.f;
#pragma unroll
      for (int i = 0; i < 8; ++i) {
        const f32x4 aq = *(const LAS f32x4*)(L + SC_ACT + w8 * 128 + i * 16);
        a0 = fmaf(aq[0], dw[4 * i], a0); a1 = fmaf(aq[1], dw[4 * i + 1], a1);
        a2 = fmaf(aq[2], dw[4 * i + 2], a2); a3 = fmaf(aq[3], dw[4 * i + 3], a3);
      }
      const float y = (a0 + a1) + (a2 + a3);
      const float yn = dpp<XOR1>(y);
      if ((l & 1) == 0) {
        const int cp = tid >> 1;
        __hip_atomic_store((GAS u64*)(ws + WS_FLAG + FL_Y2 + ((cp >> 3) * 56 + fp) * 64) + (cp & 7), (1ull << 32) | (u64)pk_bf16(y, yn), RLX_AGENT);
      }
    }
    STAMP(12);
  } else if (o2 < 11 && w8 == 4) {
    const int cb = xcd * 4 + (o2 - 7);
    unsigned whi, wlo;
    for (unsigned spins = 0;; ++spins) {
      const u64 g = __hip_atomic_load((GAS u64*)(ws + WS_FLAG + FL_WIN + cb * 128), RLX_AGENT);
      whi = __builtin_amdgcn_readfirstlane((unsigned)(g >> 32)); wlo = __builtin_amdgcn_readfirstlane((unsigned)g);
      if ((wlo >> 31) != 0u || spins > SPIN_CAP) { TMO(5); break; }
      __builtin_amdgcn_s_sleep(4);
    }
    STAMP(10);
    const int wc = wlo & 0x7f, wrow = 2047 - (int)((wlo >> 8) & 0x7ffu);
    const float sig = 1.0f / (1.0f + __expf(-unord_f32(whi)));
    GAS unsigned* done = (GAS unsigned*)(ws + WS_FLAG + FL_DONE) + wc;
    for (unsigned spins = 0;; ++spins) {
      if (__builtin_amdgcn_readfirstlane(__hip_atomic_load(done, RLX_AGENT)) != 0 || spins > SPIN_CAP) { TMO(3); break; }
      __builtin_amdgcn_s_sleep(1);
    }
    const u64 bs64 = __hip_atomic_load((GAS u64*)(ws + WS_BASE + wc * 2048 + cb * 64) + (l & 7), RLX_AGENT);
    const u32x2 bs = {(unsigned)bs64, (unsigned)(bs64 >> 32)};
    STAMP(11);
    float y0, y1;
    for (unsigned spins = 0;; ++spins) {
      u64 g[7]; unsigned ok = 1u;
#pragma unroll
      for (int i = 0; i < 7; ++i) g[i] = __hip_atomic_load((GAS u64*)(ws + WS_FLAG + FL_Y2 + cb * 56 * 64) + l + i * 64, RLX_AGENT);
      y0 = 0.f; y1 = 0.f;
#pragma unroll
      for (int i = 0; i < 7; ++i) { ok &= (unsigned)(g[i] >> 32); y0 += bf_lo((unsigned)g[i]); y1 += bf_hi((unsigned)g[i]); }
      if (__all(ok != 0u) || spins > SPIN_CAP) { TMO(4); break; }
      asm volatile("" ::: "memory");
      __builtin_amdgcn_s_sleep(1);
    }
    STAMP(13);
    y0 += dpp<XOR8>(y0); y1 += dpp<XOR8>(y1);
    float yy = fold16(y0, y1);
    yy = fold32(yy, yy);
    auto t = __builtin_amdgcn_permlane16_swap(__float_as_uint(yy), __float_as_uint(yy), false, false);
    const float f0 = __uint_as_float(t[0]), f1 = __uint_as_float(t[1]);
    if (l < 8)
      __builtin_amdgcn_raw_buffer_store_b32(pk_bf16(__uint_as_float(bs[0]) + f0 * sig, __uint_as_float(bs[1]) + f1 * sig), ro,
                                            (wrow * D_ + cb * 16 + l * 2) * 2, 0, AUX_SC1);
    STAMP(14);
  }
  STAMP(15); TSDUMP(1, w8);
}

extern "C" void kernel_launch(void* const* d_in, const int* in_sizes, int n_in, void* d_out, int out_size, void* d_ws,
                              size_t ws_size, hipStream_t stream) {
  const u16* x = (const u16*)d_in[0];
  const u16* cr = (const u16*)d_in[1];
  const u16* ci = (const u16*)d_in[2];
  const u16* dr = (const u16*)d_in[3];
  const u16* di = (const u16*)d_in[4];
  const u16* wr = (const u16*)d_in[6];
  const u16* wg = (const u16*)d_in[7];
  const u16* wu = (const u16*)d_in[8];
  const u16* wd = (const u16*)d_in[9];
  unsigned char* ws = (unsigned char*)d_ws;
  u16* out = (u16*)d_out;
  (void)hipFuncSetAttribute((const void*)k1_ln_gemm, hipFuncAttributeMaxDynamicSharedMemorySize, K1_LDS);
  (void)hipFuncSetAttribute((const void*)k2_gemm, hipFuncAttributeMaxDynamicSharedMemorySize, K2_LDS);
  k1_ln_gemm<<<256, 512, K1_LDS, stream>>>(x, dr, di, cr, ci, wg, wu, wd, ws);
  k2_gemm<<<256, 512, K2_LDS, stream>>>(cr, ci, x, wr, wg, wu, wd, ws, out);
}
```

```cpp
#include <hip/hip_runtime.h>
#include <stdint.h>

typedef unsigned short u16;
typedef short bf16x8 __attribute__((ext_vector_type(8)));
typedef __bf16 bf16x2_t __attribute__((ext_vector_type(2)));
typedef float f32x16 __attribute__((ext_vector_type(16)));
typedef float f32x4 __attribute__((ext_vector_type(4)));
typedef float f32x2 __attribute__((ext_vector_type(2)));
typedef unsigned u32x4 __attribute__((ext_vector_type(4)));
typedef unsigned u32x2 __attribute__((ext_vector_type(2)));
typedef unsigned long long u64;
#define DI __device__ __forceinline__
#define LAS __attribute__((address_space(3)))
#define GAS __attribute__((address_space(1)))
#define MFMA32(a, b, c) __builtin_amdgcn_mfma_f32_32x32x16_bf16((a), (b), (c), 0, 0, 0)
#define MFMA16(a, b, c) __builtin_amdgcn_mfma_f32_16x16x32_bf16((a), (b), (c), 0, 0, 0)
#define DMA(rs, ldsp, voff, soff, imm, aux) __builtin_amdgcn_raw_ptr_buffer_load_lds(rs, (LAS void*)(ldsp), 16, voff, soff, imm, aux)
#define WAITV(n) asm volatile("s_waitcnt vmcnt(" #n ")" ::: "memory")
#define WAITL() asm volatile("s_waitcnt lgkmcnt(0)" ::: "memory")
#define BAR() do { asm volatile("" ::: "memory"); __builtin_amdgcn_s_barrier(); asm volatile("" ::: "memory"); } while (0)
#define SB() __builtin_amdgcn_sched_barrier(0)
#define RLX_AGENT __ATOMIC_RELAXED, __HIP_MEMORY_SCOPE_AGENT
#define RSRC(p, bytes) __builtin_amdgcn_make_buffer_rsrc((void*)(p), (short)0, (int)(bytes), 0x00020000)
constexpr int AUX_NT = 2, AUX_SC1 = 16;
#define AAUX 0
#define K1AUX 0

constexpr int S_ = 2048, D_ = 512, F_ = 1792;
#define TSDECL
#define STAMP(i)
#define TSDUMP(kern, wave)

DI float bf_lo(unsigned w) { return __uint_as_float(w << 16); }
DI float bf_hi(unsigned w) { return __uint_as_float(w & 0xffff0000u); }
DI unsigned pk_bf16(float lo, float hi) {
  f32x2 v = {lo, hi};
  bf16x2_t b = __builtin_convertvector(v, bf16x2_t);
  return __builtin_bit_cast(unsigned, b);
}
DI float dot2(unsigned a, unsigned b, float acc) {
  return __builtin_amdgcn_fdot2_f32_bf16(__builtin_bit_cast(bf16x2_t, a), __builtin_bit_cast(bf16x2_t, b), acc, false);
}
template <int CTRL> DI float dpp(float x) {
  return __builtin_bit_cast(float, __builtin_amdgcn_mov_dpp(__builtin_bit_cast(int, x), CTRL, 0xf, 0xf, true));
}
template <int CTRL> DI unsigned dppu(unsigned x) { return (unsigned)__builtin_amdgcn_mov_dpp((int)x, CTRL, 0xf, 0xf, true); }
constexpr int XOR1 = 0xB1, XOR2 = 0x4E, XOR7 = 0x141, XOR8 = 0x128;
DI float fold32(float a, float b) {
  auto t = __builtin_amdgcn_permlane32_swap(__float_as_uint(a), __float_as_uint(b), false, false);
  return __uint_as_float(t[0]) + __uint_as_float(t[1]);
}
DI float fold16(float a, float b) {
  auto t = __builtin_amdgcn_permlane16_swap(__float_as_uint(a), __float_as_uint(b), false, false);
  return __uint_as_float(t[0]) + __uint_as_float(t[1]);
}
DI float bsel(unsigned m, float a, float b) { return __uint_as_float((m & __float_as_uint(a)) | (~m & __float_as_uint(b))); }
DI unsigned ord_f32(float v) { const unsigned u = __float_as_uint(v); return u ^ ((unsigned)((int)u >> 31) | 0x80000000u); }
DI float unord_f32(unsigned o) { return __uint_as_float(o ^ ((o >> 31) ? 0x80000000u : 0xffffffffu)); }
DI void max2(unsigned& hi, unsigned& lo, unsigned ohi, unsigned olo) {
  const bool t = (ohi > hi) | ((ohi == hi) & (olo > lo));
  hi = t ? ohi : hi; lo = t ? olo : lo;
}

constexpr size_t WS_AB = 0;
constexpr size_t WS_PART = WS_AB + (size_t)4096 * 512 * 2;
constexpr size_t WS_BASE = WS_PART + (size_t)256 * 32768;
constexpr size_t WS_FLAG = WS_BASE + (size_t)128 * 2048;
constexpr int FL_HN = 0;
constexpr int FL_Y2 = 128 * 2048;
constexpr int FL_AB = FL_Y2 + 32 * 56 * 64;
constexpr int FL_ARR = FL_AB + 256 * 512;
constexpr int FL_CAND = FL_ARR + 128 * 256;
constexpr int FL_WIN = FL_CAND + 8 * 1024;
constexpr int FL_DONE = FL_WIN + 32 * 128;
constexpr int FL_EPOCH = FL_DONE + 1024;
constexpr unsigned SPIN_CAP = 1u << 18;
#define TMO(code)

constexpr int LD_A = 0;
constexpr int LD_B = 65536;
constexpr int LD_P1 = 49152;
constexpr int P1_BUF = 24576;
constexpr int SC_ST = 147456;
constexpr int SC_SD = SC_ST + 2048;
constexpr int SC_PF = SC_SD + 256;
constexpr int SC_RED = SC_PF + 1024;
constexpr int SC_X = SC_RED + 2048;
constexpr int SC_GU = SC_X + 2048;
constexpr int SC_ACT = SC_GU + 2048;
constexpr int LDS_BYTES = SC_ACT + 1024;
static_assert(LDS_BYTES <= 163840, "LDS");

__global__ __launch_bounds__(512, 2) void fused_kernel(const u16* __restrict__ x, const u16* __restrict__ cr,
                                                       const u16* __restrict__ ci, const u16* __restrict__ dr,
                                                       const u16* __restrict__ di, const u16* __restrict__ wr,
                                                       const u16* __restrict__ wg_, const u16* __restrict__ wu_,
                                                       const u16* __restrict__ wd_, unsigned char* ws, u16* out) {
  extern __shared__ __attribute__((aligned(16))) unsigned char smem[];
  LAS unsigned char* L = (LAS unsigned char*)smem;
  const int tid = threadIdx.x, w8 = __builtin_amdgcn_readfirstlane(tid >> 6), w = w8 & 3, l = tid & 63, r = l & 31, h = l >> 5;
  const int b = blockIdx.x, xcd = b & 7, j = b >> 3;
  TSDECL STAMP(0);
  const unsigned epoch_v = __hip_atomic_load((GAS unsigned*)(ws + WS_FLAG + FL_EPOCH), RLX_AGENT);

  const int isB = xcd >> 2, sb = ((xcd >> 1) & 1) * 8 + (xcd & 1) * 4 + (j & 3), e8 = j >> 2;
  const u16* dm = (isB ? di : dr) + (size_t)e8 * 64 * D_;
  const auto rx = RSRC(x + (size_t)sb * 128 * D_, 128 * 1024);
  const auto rd = RSRC(dm, 64 * 1024);
  const int sz0 = (((l & 7) ^ ((l >> 4) & 7)) * 16), sz1 = (((l & 7) ^ ((4 + (l >> 4)) & 7)) * 16);
  const int v0 = (l >> 3) * 1024 + sz0, v1 = (l >> 3) * 1024 + sz1 + 7 * 1024;
  const int rot1 = (e8 + 2 * (j & 3)) & 7;
  auto issue = [&](int t_, int buf) {
    const int ko = ((t_ + rot1) & 7) * 128;
    LAS unsigned char* bx = L + LD_P1 + buf * P1_BUF + w * 4096;
    LAS unsigned char* bd = L + LD_P1 + buf * P1_BUF + 16384 + w * 2048;
    DMA(rd, bd, v0, 16 * w * 1024 + ko, 0, K1AUX);
    DMA(rd, bd, v1, 16 * w * 1024 + ko, 1024, K1AUX);
    DMA(rx, bx, v0, 32 * w * 1024 + ko, 0, K1AUX);
    DMA(rx, bx, v1, 32 * w * 1024 + ko, 1024, K1AUX);
    DMA(rx, bx, v0, 32 * w * 1024 + ko + 14 * 1024, 2048, K1AUX);
    DMA(rx, bx, v1, 32 * w * 1024 + ko + 14 * 1024, 3072, K1AUX);
  };
  issue(w8 >> 2, w8 >> 2);
  STAMP(1);
  const int ks = xcd >> 1, mb = (xcd & 1) * 8 + (j >> 2), nb = j & 3;
  const u16* A = ((ks >> 1) ? ci : cr) + (size_t)mb * 128 * S_ + (ks & 1) * 1024;
  const auto ra = RSRC(A, 127 * 4096 + 2048);
  const auto rb = RSRC(ws + WS_AB + (size_t)(nb * 64 + ks * 16) * 16384, 16 * 16384);
  const int va0 = (32 * w + (l >> 3)) * 4096 + sz0, va1 = (32 * w + (l >> 3)) * 4096 + sz1;
  const int vb = w * 4096 + l * 16;
  const int rot2 = (4 * nb + 2 * (mb & 7)) & 15;
  auto stageA = [&](int t_, int buf) {
    const int t = (t_ + rot2) & 15;
    DMA(ra, L + LD_A + buf * 16384 + w * 4096, va0, t * 128, 0, AAUX);
    DMA(ra, L + LD_A + buf * 16384 + w * 4096, va1, t * 128 + 1 * (32768 - 1024), 1024, AAUX);
    DMA(ra, L + LD_A + buf * 16384 + w * 4096, va0, t * 128 + 2 * (32768 - 1024), 2048, AAUX);
    DMA(ra, L + LD_A + buf * 16384 + w * 4096, va1, t * 128 + 3 * (32768 - 1024), 3072, AAUX);
  };
  auto stageB = [&](int t_, int buf) {
    const int t = (t_ + rot2) & 15;
    DMA(rb, L + LD_B + buf * 16384 + w * 4096, vb, t * 16384, 0, AUX_SC1);
    DMA(rb, L + LD_B + buf * 16384 + w * 4096, vb, t * 16384, 1024, AUX_SC1);
    DMA(rb, L + LD_B + buf * 16384 + w * 4096, vb, t * 16384, 2048, AUX_SC1);
    DMA(rb, L + LD_B + buf * 16384 + w * 4096, vb, t * 16384, 3072, AUX_SC1);
  };
  const int i16 = l & 15, q4 = l >> 4;
  unsigned tag;
  u64 tag64;
  auto make_tag = [&]() {
    const unsigned e = __builtin_amdgcn_readfirstlane(epoch_v);
    tag = (e + 1u) * 0x9E3779B1u ^ 0x5BD1E995u;
    tag64 = ((u64)tag << 32) | (u64)(e * 0x85EBCA77u + 0x27D4EB2Fu);
  };

  if (w8 >= 4) {
    issue(2, 2);
    make_tag();
    f32x4 sdv = {};
    const bf16x8 ones = {0x3f80, 0x3f80, 0x3f80, 0x3f80, 0x3f80, 0x3f80, 0x3f80, 0x3f80};
    const int sdd = 16384 + (w * 16 + i16) * 128;
    const int ps0 = (q4 ^ (i16 >> 1)) * 16, ps1 = ((4 + q4) ^ (i16 >> 1)) * 16;
#pragma unroll
    for (int t = 0; t < 8; ++t) {
      if (t >= 1 && t < 6) WAITV(12); else if (t == 6) WAITV(10); else if (t == 7) WAITV(8);
      WAITL();
      BAR();
      const LAS unsigned char* bb = L + LD_P1 + (t & 3) * P1_BUF;
      const bf16x8 d0 = *(const LAS bf16x8*)(bb + sdd + ps0), d1 = *(const LAS bf16x8*)(bb + sdd + ps1);
      SB();
      if (t < 5) issue(t + 3, (t + 3) & 3);
      if (t == 5) stageA(0, 0);
      if (t == 6) stageA(1, 1);
      SB();
      sdv = MFMA16(d0, ones, sdv);
      sdv = MFMA16(d1, ones, sdv);
      SB();
    }
    if (i16 == 0) *(LAS f32x4*)(L + SC_SD + (w * 16 + q4 * 4) * 4) = sdv;
    WAITL();
    BAR();
    STAMP(2);
    stageA(2, 2);
    stageA(3, 3);
    if (w8 == 4) {
      GAS u64* fl = (GAS u64*)(ws + WS_FLAG + FL_AB) + ((ks * 16 + mb) * 4 + nb) * 64 + l;
      for (unsigned spins = 0;; ++spins) {
        if (__all(__hip_atomic_load(fl, RLX_AGENT) == tag64) || spins > SPIN_CAP) { TMO(6); break; }
        __builtin_amdgcn_s_sleep(1);
      }
    }
    WAITV(0);
    BAR();
    STAMP(3);
    stageB(1, 1);
    BAR();
    stageB(2, 2); stageB(3, 3);
    WAITV(8); BAR(); stageA(4, 0); stageB(4, 0);
    WAITV(12); BAR(); stageA(5, 1); stageB(5, 1);
#pragma unroll
    for (int t = 2; t < 12; ++t) {
      WAITV(16);
      BAR();
      stageA(t + 4, t & 3); stageB(t + 4, t & 3);
    }
    WAITV(16); BAR(); WAITV(8); BAR(); WAITV(0); BAR();
    STAMP(5);
  } else {
    {
      f32x16 acc0 = {}, acc1 = {}, gram = {};
      f32x4 rsum = {};
      const short sv = (i16 == (q4 & 1)) ? (short)0x3f80 : (short)0;
      const bf16x8 selB = {sv, sv, sv, sv, sv, sv, sv, sv};
      bf16x8 fx[2][4], f0[2][4], f1[2][4];
      int pos[4];
#pragma unroll
      for (int kk = 0; kk < 4; ++kk) pos[kk] = r * 128 + (((2 * kk + h) ^ ((r >> 1) & 7)) * 16);
      auto ldf = [&](int t, int kk) {
        const LAS unsigned char* bb = L + LD_P1 + (t & 3) * P1_BUF;
        fx[t & 1][kk] = *(const LAS bf16x8*)(bb + w * 4096 + pos[kk]);
        f0[t & 1][kk] = *(const LAS bf16x8*)(bb + 16384 + pos[kk]);
        f1[t & 1][kk] = *(const LAS bf16x8*)(bb + 16384 + 4096 + pos[kk]);
      };
      make_tag();
      WAITV(0);
      BAR();
      STAMP(2);
#pragma unroll
      for (int kk = 0; kk < 4; ++kk) ldf(0, kk);
#pragma unroll
      for (int t = 0; t < 7; ++t) {
        const int p = t & 1;
        WAITL();
        BAR();
#pragma unroll
        for (int kk = 0; kk < 4; ++kk) {
          ldf(t + 1, kk);
          SB();
          gram = MFMA32(fx[p][kk], fx[p][kk], gram);
          rsum = MFMA16(fx[p][kk], selB, rsum);
          acc0 = MFMA32(fx[p][kk], f0[p][kk], acc0);
          acc1 = MFMA32(fx[p][kk], f1[p][kk], acc1);
          SB();
        }
      }
#pragma unroll
      for (int kk = 0; kk < 4; ++kk) { gram = MFMA32(fx[1][kk], fx[1][kk], gram); rsum = MFMA16(fx[1][kk], selB, rsum); }
      SB();
      acc0 = MFMA32(fx[1][0], f0[1][0], acc0); acc1 = MFMA32(fx[1][0], f1[1][0], acc1);
      acc0 = MFMA32(fx[1][1], f0[1][1], acc0); acc1 = MFMA32(fx[1][1], f1[1][1], acc1);
      SB();
      LAS float* st = (LAS float*)(L + SC_ST + w * 512);
      if (i16 < 2) *(LAS f32x4*)(st + i16 * 16 + q4 * 4) = rsum;
      float dg;
      {
        const unsigned m0 = 0u - (unsigned)(r & 1), m1 = 0u - (unsigned)((r >> 1) & 1), m3 = 0u - (unsigned)((r >> 3) & 1), m4 = 0u - (unsigned)(r >> 4);
        float e[4];
#pragma unroll
        for (int g = 0; g < 4; ++g) e[g] = bsel(m1, bsel(m0, gram[4 * g + 3], gram[4 * g + 2]), bsel(m0, gram[4 * g + 1], gram[4 * g]));
        dg = bsel(m4, bsel(m3, e[3], e[2]), bsel(m3, e[1], e[0]));
      }
      SB();
      acc0 = MFMA32(fx[1][2], f0[1][2], acc0); acc1 = MFMA32(fx[1][2], f1[1][2], acc1);
      SB();
      {
        const float mean = st[r] * (1.0f / D_);
        float rs = __builtin_amdgcn_rsqf(fmaxf(dg * (1.0f / D_) - mean * mean, 0.f) + 1e-5f);
        if (isB) rs = -rs;
        if (h == ((r >> 2) & 1)) *(LAS f32x2*)(st + 32 + 2 * r) = (f32x2){mean * rs, rs};
      }
      SB();
      acc0 = MFMA32(fx[1][3], f0[1][3], acc0); acc1 = MFMA32(fx[1][3], f1[1][3], acc1);
      SB();
      WAITL();
      BAR();
      STAMP(16);
      const LAS float* sdl = (const LAS float*)(L + SC_SD);
      const float cs0 = sdl[r], cs1 = sdl[32 + r];
      const LAS f32x4* st4 = (const LAS f32x4*)(st + 32);
      unsigned p0[8], p1[8];
#pragma unroll
      for (int g = 0; g < 4; ++g) {
        const f32x4 sa = st4[4 * g + 2 * h], sbv = st4[4 * g + 2 * h + 1];
        p0[2 * g] = pk_bf16(acc0[4 * g] * sa[1] - sa[0] * cs0, acc0[4 * g + 1] * sa[3] - sa[2] * cs0);
        p0[2 * g + 1] = pk_bf16(acc0[4 * g + 2] * sbv[1] - sbv[0] * cs0, acc0[4 * g + 3] * sbv[3] - sbv[2] * cs0);
        p1[2 * g] = pk_bf16(acc1[4 * g] * sa[1] - sa[0] * cs1, acc1[4 * g + 1] * sa[3] - sa[2] * cs1);
        p1[2 * g + 1] = pk_bf16(acc1[4 * g + 2] * sbv[1] - sbv[0] * cs1, acc1[4 * g + 3] * sbv[3] - sbv[2] * cs1);
      }
      const int kk0 = isB * 2048 + sb * 128 + w * 32;
      const int kb = kk0 >> 6, kst = (kk0 >> 4) & 3;
      const int nbp = e8 >> 1, nt0 = (e8 & 1) * 2;
      const auto rab = RSRC(ws + WS_AB + (size_t)(nbp * 64 + kb) * 16384, 16384);
#pragma unroll
      for (int s2 = 0; s2 < 2; ++s2) {
        {
          auto t0 = __builtin_amdgcn_permlane32_swap(p0[4 * s2], p0[4 * s2 + 2], false, false);
          auto t1 = __builtin_amdgcn_permlane32_swap(p0[4 * s2 + 1], p0[4 * s2 + 3], false, false);
          u32x4 o = {t0[0], t1[0], t0[1], t1[1]};
          __builtin_amdgcn_raw_buffer_store_b128(o, rab, l * 16, ((kst + s2) * 4 + nt0) * 1024, AUX_SC1);
        }
        {
          auto t0 = __builtin_amdgcn_permlane32_swap(p1[4 * s2], p1[4 * s2 + 2], false, false);
          auto t1 = __builtin_amdgcn_permlane32_swap(p1[4 * s2 + 1], p1[4 * s2 + 3], false, false);
          u32x4 o = {t0[0], t1[0], t0[1], t1[1]};
          __builtin_amdgcn_raw_buffer_store_b128(o, rab, l * 16, ((kst + s2) * 4 + nt0 + 1) * 1024, AUX_SC1);
        }
      }
      STAMP(17);
      WAITV(0);
      if (l < 16)
        __hip_atomic_store((GAS u64*)(ws + WS_FLAG + FL_AB) + (((isB * 2 + (sb >> 3)) * 16 + l) * 4 + nbp) * 64 + ((sb & 7) * 2 + (e8 & 1)) * 4 + w,
                           tag64, RLX_AGENT);
      STAMP(18);
    }
    const int wm = w >> 1, wn = w & 1;
    f32x16 acc[2][2] = {};
    int aoff[4];
    const int sw = (r >> 1) & 7;
#pragma unroll
    for (int kk = 0; kk < 4; ++kk) aoff[kk] = LD_A + (wm * 64 + r) * 128 + (((2 * kk + h) ^ sw) * 16);
    const int boff = LD_B + wn * 2048 + l * 16;
    bf16x8 fa[2][2], fb[2][2];
    auto ldf = [&](int t, int kk, int p) {
      const int bo = (t & 3) * 16384;
      fb[p][0] = *(const LAS bf16x8*)(L + bo + boff + kk * 4096);
      fa[p][0] = *(const LAS bf16x8*)(L + bo + aoff[kk]);
      fa[p][1] = *(const LAS bf16x8*)(L + bo + aoff[kk] + 4096);
      fb[p][1] = *(const LAS bf16x8*)(L + bo + boff + kk * 4096 + 1024);
    };
    auto mma = [&](int p) {
      acc[0][0] = MFMA32(fb[p][0], fa[p][0], acc[0][0]);
      acc[1][0] = MFMA32(fb[p][0], fa[p][1], acc[1][0]);
      acc[0][1] = MFMA32(fb[p][1], fa[p][0], acc[0][1]);
      acc[1][1] = MFMA32(fb[p][1], fa[p][1], acc[1][1]);
      SB();
    };
    BAR();
    STAMP(3);
    stageB(0, 0);
    WAITV(0);
    BAR();
    STAMP(4);
    ldf(0, 0, 0);
#pragma unroll
    for (int t = 0; t < 16; ++t) {
      ldf(t, 1, 1); SB(); mma(0);
      ldf(t, 2, 0); SB(); mma(1);
      ldf(t, 3, 1); SB(); mma(0);
      if (t < 15) { WAITL(); BAR(); ldf(t + 1, 0, 0); SB(); }
      mma(1);
    }
    STAMP(5);
    const auto rp = RSRC(ws + WS_PART + (size_t)((ks * 16 + mb) * 4 + nb) * 32768, 32768);
#pragma unroll
    for (int nt = 0; nt < 2; ++nt)
#pragma unroll
      for (int mt = 0; mt < 2; ++mt)
#pragma unroll
        for (int s2 = 0; s2 < 2; ++s2) {
          const f32x16& a = acc[mt][nt];
          const unsigned p0 = pk_bf16(a[8 * s2], a[8 * s2 + 1]), p1 = pk_bf16(a[8 * s2 + 2], a[8 * s2 + 3]);
          const unsigned p2 = pk_bf16(a[8 * s2 + 4], a[8 * s2 + 5]), p3 = pk_bf16(a[8 * s2 + 6], a[8 * s2 + 7]);
          auto t0 = __builtin_amdgcn_permlane32_swap(p0, p2, false, false);
          auto t1 = __builtin_amdgcn_permlane32_swap(p1, p3, false, false);
          const u32x4 o = {t0[0], t1[0], t0[1], t1[1]};
          __builtin_amdgcn_raw_buffer_store_b128(o, rp, (((w * 2 + mt) * 2 + nt) * 2 + s2) * 1024 + l * 16, 0, AUX_SC1);
          SB();
        }
    STAMP(6);
    WAITV(0);
    if (l < 4)
      __hip_atomic_store((GAS u64*)(ws + WS_FLAG + FL_ARR) + (mb * 8 + wm * 4 + l) * 32 + (ks * 4 + nb) * 2 + wn, tag64, RLX_AGENT);
    STAMP(7);
  }
  GAS u64* cand = (GAS u64*)(ws + WS_FLAG + FL_CAND);
  const auto ro = RSRC(out, S_ * D_ * 2);
  const auto rbase = RSRC(ws + WS_BASE, 128 * 2048);
  const u64 thi = (u64)tag << 32;

  if ((j & 1) == 0) {
    const int q2 = ks * 2 + (nb >> 1), ci_ = mb * 8 + q2;
    const int r16 = l & 15, c = tid >> 4;
    const int row = ci_ * 16 + r16;
    BAR();
    u32x4 xv[2], wv[2];
#pragma unroll
    for (int u = 0; u < 2; ++u) {
      xv[u] = *(const u32x4*)(x + (size_t)row * D_ + c * 8 + u * 256);
      wv[u] = *(const u32x4*)(wr + c * 8 + u * 256);
    }
    if (w8 == 5) {
      GAS u64* arr = (GAS u64*)(ws + WS_FLAG + FL_ARR) + ci_ * 32 + r;
      for (unsigned spins = 0;; ++spins) {
        if (__all(__hip_atomic_load(arr, RLX_AGENT) == tag64) || spins > SPIN_CAP) { TMO(0); break; }
        __builtin_amdgcn_s_sleep(1);
      }
    }
    const auto rp = RSRC(ws + WS_PART, 256 * 32768);
    const int poff = (mb * 4 + (c >> 4)) * 32768 +
                     (((((q2 >> 2) * 2 + ((c >> 3) & 1)) * 2 + ((q2 >> 1) & 1)) * 2 + ((c >> 2) & 1)) * 2 + ((c >> 1) & 1)) * 1024 +
                     ((c & 1) * 32 + (q2 & 1) * 16 + r16) * 16;
    SB();
    BAR();
    STAMP(8);
    u32x4 pv[2][4];
#pragma unroll
    for (int u = 0; u < 2; ++u)
#pragma unroll
      for (int k2 = 0; k2 < 4; ++k2) pv[u][k2] = __builtin_amdgcn_raw_buffer_load_b128(rp, poff, (k2 * 64 + u * 2) * 32768, AUX_SC1);
    float sw = 0.f, wf[16], hv[16];
#pragma unroll
    for (int u = 0; u < 2; ++u)
#pragma unroll
      for (int i = 0; i < 4; ++i) {
        wf[8 * u + 2 * i] = bf_lo(wv[u][i]); wf[8 * u + 2 * i + 1] = bf_hi(wv[u][i]);
        sw += wf[8 * u + 2 * i] + wf[8 * u + 2 * i + 1];
        hv[8 * u + 2 * i] = bf_lo(xv[u][i]) + ((bf_lo(pv[u][0][i]) + bf_lo(pv[u][1][i])) + (bf_lo(pv[u][2][i]) + bf_lo(pv[u][3][i])));
        hv[8 * u + 2 * i + 1] = bf_hi(xv[u][i]) + ((bf_hi(pv[u][0][i]) + bf_hi(pv[u][1][i])) + (bf_hi(pv[u][2][i]) + bf_hi(pv[u][3][i])));
      }
    float mu = 0.f, m2 = 0.f, hw = 0.f;
#pragma unroll
    for (int i = 0; i < 4; ++i) mu += (hv[4 * i] + hv[4 * i + 1]) + (hv[4 * i + 2] + hv[4 * i + 3]);
    mu *= 0.0625f;
    STAMP(9);
#pragma unroll
    for (int i = 0; i < 16; ++i) { const float d = hv[i] - mu; m2 = fmaf(d, d, m2); hw = fmaf(hv[i], wf[i], hw); }
    float hs = fold16(hw, sw);
    {
      auto t = __builtin_amdgcn_permlane16_swap(__float_as_uint(mu), __float_as_uint(mu), false, false);
      const float a = __uint_as_float(t[0]), bb = __uint_as_float(t[1]), d = a - bb;
      m2 = fold16(m2, m2) + d * d * 8.0f; mu = (a + bb) * 0.5f;
    }
    hs = fold32(hs, hs);
    {
      auto t = __builtin_amdgcn_permlane32_swap(__float_as_uint(mu), __float_as_uint(mu), false, false);
      const float a = __uint_as_float(t[0]), bb = __uint_as_float(t[1]), d = a - bb;
      m2 = fold32(m2, m2) + d * d * 16.0f; mu = (a + bb) * 0.5f;
    }
    LAS float* red = (LAS float*)(L + SC_RED);
    if (l < 16) *(LAS f32x2*)(red + (l * 8 + w8) * 4) = (f32x2){mu, m2};
    if (l < 32) red[(r16 * 8 + w8) * 4 + 2 + (l >> 4)] = hs;
    WAITL();
    BAR();
    STAMP(10);
    float mean = 0.f, M2 = 0.f, HW = 0.f, SW = 0.f;
    {
      f32x4 t[8];
#pragma unroll
      for (int i = 0; i < 8; ++i) t[i] = *(const LAS f32x4*)(red + (r16 * 8 + i) * 4);
#pragma unroll
      for (int i = 0; i < 8; ++i) { mean += t[i][0]; M2 += t[i][1]; HW += t[i][2]; SW += t[i][3]; }
      mean *= 0.125f;
#pragma unroll
      for (int i = 0; i < 8; ++i) { const float d = t[i][0] - mean; M2 = fmaf(d * d, 64.0f, M2); }
    }
    const float rstd = __builtin_amdgcn_rsqf(M2 * (1.0f / D_) + 1e-5f);
    const float rr = (HW - mean * SW) * rstd;
    unsigned key = (ord_f32(rr) & ~15u) | (unsigned)(15 - r16);
    key = max(key, dppu<XOR1>(key)); key = max(key, dppu<XOR2>(key));
    key = max(key, dppu<XOR7>(key)); key = max(key, dppu<XOR8>(key));
    if (tid < 8) __hip_atomic_store(cand + tid * 128 + ci_, thi | (u64)key, RLX_AGENT);
    STAMP(11);
    const bool best = (15 - (int)(key & 15u)) == r16;
#pragma unroll
    for (int u = 0; u < 2; ++u) {
      float hn[8], ob[8];
#pragma unroll
      for (int i = 0; i < 8; ++i) { hn[i] = (hv[8 * u + i] - mean) * rstd; ob[i] = hv[8 * u + i] + hn[i]; }
      const u32x4 o = {pk_bf16(ob[0], ob[1]), pk_bf16(ob[2], ob[3]), pk_bf16(ob[4], ob[5]), pk_bf16(ob[6], ob[7])};
      const int cc = c + 32 * u;
      if (best) {
        GAS u64* hp = (GAS u64*)(ws + WS_FLAG + FL_HN + ci_ * 2048) + cc * 4;
#pragma unroll
        for (int i = 0; i < 4; ++i) __hip_atomic_store(hp + i, thi | (u64)pk_bf16(hn[2 * i], hn[2 * i + 1]), RLX_AGENT);
        const u32x4 b0 = {__float_as_uint(ob[0]), __float_as_uint(ob[1]), __float_as_uint(ob[2]), __float_as_uint(ob[3])};
        const u32x4 b1 = {__float_as_uint(ob[4]), __float_as_uint(ob[5]), __float_as_uint(ob[6]), __float_as_uint(ob[7])};
        __builtin_amdgcn_raw_buffer_store_b128(b0, rbase, ci_ * 2048 + cc * 32, 0, AUX_SC1);
        __builtin_amdgcn_raw_buffer_store_b128(b1, rbase, ci_ * 2048 + cc * 32 + 16, 0, AUX_SC1);
        __builtin_amdgcn_raw_buffer_store_b128(o, ro, (row * D_ + cc * 8) * 2, 0, AUX_SC1);
      } else {
        __builtin_amdgcn_raw_buffer_store_b128(o, ro, (row * D_ + cc * 8) * 2, 0, 0);
      }
    }
    WAITV(0);
    BAR();
    if (tid == 0) __hip_atomic_store((GAS u64*)(ws + WS_FLAG + FL_DONE) + ci_, tag64, RLX_AGENT);
    STAMP(12);
    TSDUMP(1, w8);
    return;
  }
  const int o2 = j >> 1;
  if (o2 < 7) {
    const int fp = xcd * 7 + o2;
    BAR();
    {
      const auto rg = RSRC(wg_, D_ * F_ * 2);
      const auto ru = RSRC(wu_, D_ * F_ * 2);
      const auto rw = RSRC(wd_ + (size_t)fp * 32 * D_, 32 * D_ * 2);
      const int vg = (w8 * 64 + (l >> 2)) * (F_ * 2) + fp * 64 + (l & 3) * 16;
#pragma unroll
      for (int i = 0; i < 4; ++i) {
        DMA(rg, L + (w8 * 4 + i) * 1024, vg, i * 16 * F_ * 2, 0, 0);
        DMA(ru, L + 32768 + (w8 * 4 + i) * 1024, vg, i * 16 * F_ * 2, 0, 0);
      }
#pragma unroll
      for (int i = 0; i < 4; ++i) DMA(rw, L + 65536 + (w8 * 4 + i) * 1024, l * 16, (w8 * 4 + i) * 1024, 0, 0);
    }
    STAMP(8);
    float gw[64], dw[32];
    WAITV(4);
    {
      const LAS u16* pg = (const LAS u16*)(L + h * 32768 + w8 * 4096 + r * 2);
#pragma unroll
      for (int i = 0; i < 64; ++i) gw[i] = __uint_as_float((unsigned)pg[i * 32] << 16);
#pragma unroll
      for (int i = 0; i < 64; ++i) asm volatile("" : "+v"(gw[i]));
    }
    WAITV(0);
    BAR();
    {
      const LAS u16* pd = (const LAS u16*)(L + 65536 + tid * 2);
#pragma unroll
      for (int i = 0; i < 32; ++i) dw[i] = __uint_as_float((unsigned)pd[i * 512] << 16);
#pragma unroll
      for (int i = 0; i < 32; ++i) asm volatile("" : "+v"(dw[i]));
    }
    STAMP(9);
    LAS float* xs = (LAS float*)(L + SC_X);
    if (w8 == 4) {
      unsigned bhi, blo;
      GAS u64* cp = cand + xcd * 128 + l;
      for (unsigned spins = 0;; ++spins) {
        const u64 g0 = __hip_atomic_load(cp, RLX_AGENT), g1 = __hip_atomic_load(cp + 64, RLX_AGENT);
        const unsigned k0 = (unsigned)g0, k1 = (unsigned)g1;
        bhi = k0 & ~15u; blo = (unsigned)((127 - l) * 16) | (k0 & 15u);
        max2(bhi, blo, k1 & ~15u, (unsigned)((63 - l) * 16) | (k1 & 15u));
        if (__all(((unsigned)(g0 >> 32) == tag) & ((unsigned)(g1 >> 32) == tag)) || spins > SPIN_CAP) { TMO(1); break; }
        __builtin_amdgcn_s_sleep(2);
      }
      max2(bhi, blo, dppu<XOR1>(bhi), dppu<XOR1>(blo));
      max2(bhi, blo, dppu<XOR2>(bhi), dppu<XOR2>(blo));
      max2(bhi, blo, dppu<XOR7>(bhi), dppu<XOR7>(blo));
      max2(bhi, blo, dppu<XOR8>(bhi), dppu<XOR8>(blo));
      {
        auto th = __builtin_amdgcn_permlane16_swap(bhi, bhi, false, false);
        auto tl = __builtin_amdgcn_permlane16_swap(blo, blo, false, false);
        bhi = th[0]; blo = tl[0]; max2(bhi, blo, th[1], tl[1]);
      }
      {
        auto th = __builtin_amdgcn_permlane32_swap(bhi, bhi, false, false);
        auto tl = __builtin_amdgcn_permlane32_swap(blo, blo, false, false);
        bhi = th[0]; blo = tl[0]; max2(bhi, blo, th[1], tl[1]);
      }
      STAMP(10);
      if (o2 == 0 && l < 8)
        __hip_atomic_store((GAS u64*)(ws + WS_FLAG + FL_WIN + (xcd * 4 + (l >> 1)) * 128) + (l & 1), thi | (u64)((l & 1) ? blo : bhi), RLX_AGENT);
      const int wc = 127 - (int)(__builtin_amdgcn_readfirstlane(blo) >> 4);
      u32x4 ga, gb;
      GAS u64* hp = (GAS u64*)(ws + WS_FLAG + FL_HN + wc * 2048) + l * 4;
      for (unsigned spins = 0;; ++spins) {
        const u64 g0 = __hip_atomic_load(hp, RLX_AGENT), g1 = __hip_atomic_load(hp + 1, RLX_AGENT);
        const u64 g2 = __hip_atomic_load(hp + 2, RLX_AGENT), g3 = __hip_atomic_load(hp + 3, RLX_AGENT);
        ga = (u32x4){(unsigned)g0 << 16, (unsigned)g0 & 0xffff0000u, (unsigned)g1 << 16, (unsigned)g1 & 0xffff0000u};
        gb = (u32x4){(unsigned)g2 << 16, (unsigned)g2 & 0xffff0000u, (unsigned)g3 << 16, (unsigned)g3 & 0xffff0000u};
        const bool ok = ((unsigned)(g0 >> 32) == tag) & ((unsigned)(g1 >> 32) == tag) & ((unsigned)(g2 >> 32) == tag) & ((unsigned)(g3 >> 32) == tag);
        if (__all(ok) || spins > SPIN_CAP) { TMO(2); break; }
        __builtin_amdgcn_s_sleep(1);
      }
      *(LAS u32x4*)(xs + l * 8) = ga;
      *(LAS u32x4*)(xs + l * 8 + 4) = gb;
    }
    WAITL();
    BAR();
    STAMP(11);
    {
      float a0 = 0.f, a1 = 0.f, a2 = 0.f, a3 = 0.f;
#pragma unroll
      for (int i = 0; i < 16; ++i) {
        const f32x4 xq = *(const LAS f32x4*)(xs + w8 * 64 + i * 4);
        a0 = fmaf(xq[0], gw[4 * i], a0); a1 = fmaf(xq[1], gw[4 * i + 1], a1);
        a2 = fmaf(xq[2], gw[4 * i + 2], a2); a3 = fmaf(xq[3], gw[4 * i + 3], a3);
      }
      ((LAS float*)(L + SC_GU))[w8 * 64 + l] = (a0 + a1) + (a2 + a3);
    }
    WAITL();
    BAR();
    {
      const LAS float* gu = (const LAS float*)(L + SC_GU) + l;
      const float s = ((gu[0] + gu[64]) + (gu[128] + gu[192])) + ((gu[256] + gu[320]) + (gu[384] + gu[448]));
      auto t = __builtin_amdgcn_permlane32_swap(__float_as_uint(s), __float_as_uint(s), false, false);
      const float g = __uint_as_float(t[0]), u = __uint_as_float(t[1]);
      if (l < 32) ((LAS float*)(L + SC_ACT))[w8 * 32 + l] = g * __builtin_amdgcn_rcpf(1.0f + __expf(-g)) * u;
    }
    WAITL();
    {
      float a0 = 0.f, a1 = 0.f, a2 = 0.f, a3 = 0.f;
#pragma unroll
      for (int i = 0; i < 8; ++i) {
        const f32x4 aq = *(const LAS f32x4*)(L + SC_ACT + w8 * 128 + i * 16);
        a0 = fmaf(aq[0], dw[4 * i], a0); a1 = fmaf(aq[1], dw[4 * i + 1], a1);
        a2 = fmaf(aq[2], dw[4 * i + 2], a2); a3 = fmaf(aq[3], dw[4 * i + 3], a3);
      }
      const float y = (a0 + a1) + (a2 + a3);
      const float yn = dpp<XOR1>(y);
      if ((l & 1) == 0) {
        const int cp = tid >> 1;
        __hip_atomic_store((GAS u64*)(ws + WS_FLAG + FL_Y2 + ((cp >> 3) * 56 + fp) * 64) + (cp & 7), thi | (u64)pk_bf16(y, yn), RLX_AGENT);
      }
    }
    STAMP(12);
  } else if (o2 < 11 && w8 == 4) {
    const int cb = xcd * 4 + (o2 - 7);
    unsigned whi, wlo;
    for (unsigned spins = 0;; ++spins) {
      const u64 g = __hip_atomic_load((GAS u64*)(ws + WS_FLAG + FL_WIN + cb * 128) + (l & 1), RLX_AGENT);
      whi = __builtin_amdgcn_readlane((unsigned)g, 0); wlo = __builtin_amdgcn_readlane((unsigned)g, 1);
      if (__all((unsigned)(g >> 32) == tag) || spins > SPIN_CAP) { TMO(5); break; }
      __builtin_amdgcn_s_sleep(4);
    }
    STAMP(10);
    const int wrow = 2047 - (int)(wlo & 0x7ffu), wc = wrow >> 4;
    const float sig = 1.0f / (1.0f + __expf(-unord_f32(whi)));
    GAS u64* done = (GAS u64*)(ws + WS_FLAG + FL_DONE) + wc;
    for (unsigned spins = 0;; ++spins) {
      if (__all(__hip_atomic_load(done, RLX_AGENT) == tag64) || spins > SPIN_CAP) { TMO(3); break; }
      __builtin_amdgcn_s_sleep(1);
    }
    const u64 bs64 = __hip_atomic_load((GAS u64*)(ws + WS_BASE + wc * 2048 + cb * 64) + (l & 7), RLX_AGENT);
    STAMP(11);
    float y0, y1;
    for (unsigned spins = 0;; ++spins) {
      u64 g[7]; bool ok = true;
#pragma unroll
      for (int i = 0; i < 7; ++i) g[i] = __hip_atomic_load((GAS u64*)(ws + WS_FLAG + FL_Y2 + cb * 56 * 64) + l + i * 64, RLX_AGENT);
      y0 = 0.f; y1 = 0.f;
#pragma unroll
      for (int i = 0; i < 7; ++i) { ok &= (unsigned)(g[i] >> 32) == tag; y0 += bf_lo((unsigned)g[i]); y1 += bf_hi((unsigned)g[i]); }
      if (__all(ok) || spins > SPIN_CAP) { TMO(4); break; }
      __builtin_amdgcn_s_sleep(1);
    }
    STAMP(13);
    y0 += dpp<XOR8>(y0); y1 += dpp<XOR8>(y1);
    float yy = fold16(y0, y1);
    yy = fold32(yy, yy);
    auto t = __builtin_amdgcn_permlane16_swap(__float_as_uint(yy), __float_as_uint(yy), false, false);
    const float f0 = __uint_as_float(t[0]), f1 = __uint_as_float(t[1]);
    if (l < 8)
      __builtin_amdgcn_raw_buffer_store_b32(pk_bf16(__uint_as_float((unsigned)bs64) + f0 * sig, __uint_as_float((unsigned)(bs64 >> 32)) + f1 * sig),
                                            ro, (wrow * D_ + cb * 16 + l * 2) * 2, 0, AUX_SC1);
    if (cb == 0 && l == 0) __hip_atomic_store((GAS unsigned*)(ws + WS_FLAG + FL_EPOCH), epoch_v + 1u, RLX_AGENT);
    STAMP(14);
  }
  STAMP(15); TSDUMP(1, w8);
}

extern "C" void kernel_launch(void* const* d_in, const int* in_sizes, int n_in, void* d_out, int out_size, void* d_ws,
                              size_t ws_size, hipStream_t stream) {
  const u16* x = (const u16*)d_in[0];
  const u16* cr = (const u16*)d_in[1];
  const u16* ci = (const u16*)d_in[2];
  const u16* dr = (const u16*)d_in[3];
  const u16* di = (const u16*)d_in[4];
  const u16* wr = (const u16*)d_in[6];
  const u16* wg = (const u16*)d_in[7];
  const u16* wu = (const u16*)d_in[8];
  const u16* wd = (const u16*)d_in[9];
  (void)hipFuncSetAttribute((const void*)fused_kernel, hipFuncAttributeMaxDynamicSharedMemorySize, LDS_BYTES);
  fused_kernel<<<256, 512, LDS_BYTES, stream>>>(x, cr, ci, dr, di, wr, wg, wu, wd, (unsigned char*)d_ws, (u16*)d_out);
}
```

```cpp
#include <hip/hip_runtime.h>
#include <stdint.h>

typedef unsigned short u16;
typedef short bf16x8 __attribute__((ext_vector_type(8)));
typedef __bf16 bf16x2_t __attribute__((ext_vector_type(2)));
typedef float f32x16 __attribute__((ext_vector_type(16)));
typedef float f32x4 __attribute__((ext_vector_type(4)));
typedef float f32x2 __attribute__((ext_vector_type(2)));
typedef unsigned u32x4 __attribute__((ext_vector_type(4)));
typedef unsigned u32x2 __attribute__((ext_vector_type(2)));
typedef unsigned long long u64;
#define DI __device__ __forceinline__
#define LAS __attribute__((address_space(3)))
#define GAS __attribute__((address_space(1)))
#define MFMA32(a, b, c) __builtin_amdgcn_mfma_f32_32x32x16_bf16((a), (b), (c), 0, 0, 0)
#define MFMA16(a, b, c) __builtin_amdgcn_mfma_f32_16x16x32_bf16((a), (b), (c), 0, 0, 0)
#define DMA(rs, ldsp, voff, soff, imm, aux) __builtin_amdgcn_raw_ptr_buffer_load_lds(rs, (LAS void*)(ldsp), 16, voff, soff, imm, aux)
#define WAITV(n) asm volatile("s_waitcnt vmcnt(" #n ")" ::: "memory")
#define WAITL() asm volatile("s_waitcnt lgkmcnt(0)" ::: "memory")
#define BAR() do { asm volatile("" ::: "memory"); __builtin_amdgcn_s_barrier(); asm volatile("" ::: "memory"); } while (0)
#define SB() __builtin_amdgcn_sched_barrier(0)
#define RLX_AGENT __ATOMIC_RELAXED, __HIP_MEMORY_SCOPE_AGENT
#define RSRC(p, bytes) __builtin_amdgcn_make_buffer_rsrc((void*)(p), (short)0, (int)(bytes), 0x00020000)
constexpr int AUX_NT = 2, AUX_SC1 = 16;
#define AAUX 0
#define BAUX AUX_SC1
#define OUTAUX AUX_SC1
#define FINAL_NAP 1
#define LIGHT_TH 64
#define LIGHT_SL 8
#define WGFLAG 3
#define SPEC_TH 96
#define K1AUX 0

constexpr int S_ = 2048, D_ = 512, F_ = 1792;
#define TSDECL
#define STAMP(i)
#define TSDUMP(kern, wave)

DI float bf_lo(unsigned w) { return __uint_as_float(w << 16); }
DI float bf_hi(unsigned w) { return __uint_as_float(w & 0xffff0000u); }
DI unsigned pk_bf16(float lo, float hi) {
  f32x2 v = {lo, hi};
  bf16x2_t b = __builtin_convertvector(v, bf16x2_t);
  return __builtin_bit_cast(unsigned, b);
}
DI float dot2(unsigned a, unsigned b, float acc) {
  return __builtin_amdgcn_fdot2_f32_bf16(__builtin_bit_cast(bf16x2_t, a), __builtin_bit_cast(bf16x2_t, b), acc, false);
}
template <int CTRL> DI float dpp(float x) {
  return __builtin_bit_cast(float, __builtin_amdgcn_mov_dpp(__builtin_bit_cast(int, x), CTRL, 0xf, 0xf, true));
}
template <int CTRL> DI unsigned dppu(unsigned x) { return (unsigned)__builtin_amdgcn_mov_dpp((int)x, CTRL, 0xf, 0xf, true); }
constexpr int XOR1 = 0xB1, XOR2 = 0x4E, XOR7 = 0x141, XOR8 = 0x128;
DI float fold32(float a, float b) {
  auto t = __builtin_amdgcn_permlane32_swap(__float_as_uint(a), __float_as_uint(b), false, false);
  return __uint_as_float(t[0]) + __uint_as_float(t[1]);
}
DI float fold16(float a, float b) {
  auto t = __builtin_amdgcn_permlane16_swap(__float_as_uint(a), __float_as_uint(b), false, false);
  return __uint_as_float(t[0]) + __uint_as_float(t[1]);
}
DI float bsel(unsigned m, float a, float b) { return __uint_as_float((m & __float_as_uint(a)) | (~m & __float_as_uint(b))); }
DI unsigned ord_f32(float v) { const unsigned u = __float_as_uint(v); return u ^ ((unsigned)((int)u >> 31) | 0x80000000u); }
DI float unord_f32(unsigned o) { return __uint_as_float(o ^ ((o >> 31) ? 0x80000000u : 0xffffffffu)); }
DI void max2(unsigned& hi, unsigned& lo, unsigned ohi, unsigned olo) {
  const bool t = (ohi > hi) | ((ohi == hi) & (olo > lo));
  hi = t ? ohi : hi; lo = t ? olo : lo;
}

DI void best_candidate(u64 c0, u64 c1, unsigned tag, unsigned& key_out, int& slot, int& nin) {
  const bool v0 = (unsigned)(c0 >> 32) == tag, v1 = (unsigned)(c1 >> 32) == tag;
  const unsigned k0 = v0 ? (unsigned)c0 : 0u, k1 = v1 ? (unsigned)c1 : 0u;
  unsigned m = max(k0, k1);
  m = max(m, dppu<XOR1>(m)); m = max(m, dppu<XOR2>(m)); m = max(m, dppu<XOR7>(m)); m = max(m, dppu<XOR8>(m));
  { auto t = __builtin_amdgcn_permlane16_swap(m, m, false, false); m = max(t[0], t[1]); }
  { auto t = __builtin_amdgcn_permlane32_swap(m, m, false, false); m = max(t[0], t[1]); }
  const u64 e0 = __ballot(k0 == m), e1 = __ballot(k1 == m);
  slot = e0 ? __builtin_ctzll(e0) : 64 + __builtin_ctzll(e1);
  key_out = __builtin_amdgcn_readfirstlane(m);
  nin = __builtin_popcountll(__ballot(v0)) + __builtin_popcountll(__ballot(v1));
}
typedef short s16x4 __attribute__((ext_vector_type(4)));
DI bf16x8 tr_frag(const LAS unsigned char* p0, const LAS unsigned char* p1) {
  const s16x4 a = __builtin_amdgcn_ds_read_tr16_b64_v4i16((LAS s16x4*)p0), b = __builtin_amdgcn_ds_read_tr16_b64_v4i16((LAS s16x4*)p1);
  return __builtin_shufflevector(a, b, 0, 1, 2, 3, 4, 5, 6, 7);
}
constexpr size_t WS_AB = 0;
constexpr size_t WS_PART = WS_AB + (size_t)4096 * 512 * 2;
constexpr size_t WS_BASE = WS_PART + (size_t)256 * 32768;
constexpr size_t WS_FLAG = WS_BASE + (size_t)128 * 4096;
constexpr int FL_HN = 0;
constexpr int FL_Y2 = 128 * 2048;
constexpr int FL_AB = FL_Y2 + 32 * 56 * 64;
constexpr int FL_ARR = FL_AB + 256 * 512;
constexpr int FL_CAND = FL_ARR + 128 * 256;
constexpr int FL_DONE = FL_CAND + 16 * 1024;
constexpr int FL_EPOCH = FL_DONE + 128 * 128;
constexpr unsigned SPIN_CAP = 1u << 18;
#define TMO(code)

constexpr int LD_A = 0;
constexpr int LD_B = 65536;
constexpr int LD_P1 = 49152;
constexpr int P1_BUF = 24576;
constexpr int SC_ST = 147456;
constexpr int SC_SD = SC_ST + 2048;
constexpr int SC_PF = SC_SD + 256;
constexpr int SC_RED = SC_PF + 1024;
constexpr int SC_X = SC_RED + 2048;
constexpr int SC_GU = SC_X + 2048;
constexpr int SC_ACT = SC_GU + 2048;
constexpr int SC_CTL = SC_ACT + 1024;
constexpr int LDS_BYTES = SC_CTL + 64;
static_assert(LDS_BYTES <= 163840, "LDS");

__global__ __launch_bounds__(512, 2) void fused_kernel(const u16* __restrict__ x, const u16* __restrict__ cr,
                                                       const u16* __restrict__ ci, const u16* __restrict__ dr,
                                                       const u16* __restrict__ di, const u16* __restrict__ wr,
                                                       const u16* __restrict__ wg_, const u16* __restrict__ wu_,
                                                       const u16* __restrict__ wd_, unsigned char* ws, u16* out) {
  extern __shared__ __attribute__((aligned(16))) unsigned char smem[];
  LAS unsigned char* L = (LAS unsigned char*)smem;
  const int tid = threadIdx.x, w8 = __builtin_amdgcn_readfirstlane(tid >> 6), w = w8 & 3, l = tid & 63, r = l & 31, h = l >> 5;
  const int b = blockIdx.x, xcd = b & 7, j = b >> 3;
  TSDECL STAMP(0);
  const unsigned epoch_v = __hip_atomic_load((GAS unsigned*)(ws + WS_FLAG + FL_EPOCH), RLX_AGENT);

  const int isB = xcd >> 2, sb = ((xcd >> 1) & 1) * 8 + (xcd & 1) * 4 + (j & 3), e8 = j >> 2;
  const u16* dm = (isB ? di : dr) + (size_t)e8 * 64 * D_;
  const auto rx = RSRC(x + (size_t)sb * 128 * D_, 128 * 1024);
  const auto rd = RSRC(dm, 64 * 1024);
  const int sz0 = (((l & 7) ^ ((l >> 4) & 7)) * 16), sz1 = (((l & 7) ^ ((4 + (l >> 4)) & 7)) * 16);
  const int v0 = (l >> 3) * 1024 + sz0, v1 = (l >> 3) * 1024 + sz1 + 7 * 1024;
  const int rot1 = (e8 + 2 * (j & 3)) & 7;
  auto issue = [&](int t_, int buf) {
    const int ko = ((t_ + rot1) & 7) * 128;
    LAS unsigned char* bx = L + LD_P1 + buf * P1_BUF + w * 4096;
    LAS unsigned char* bd = L + LD_P1 + buf * P1_BUF + 16384 + w * 2048;
    DMA(rd, bd, v0, 16 * w * 1024 + ko, 0, K1AUX);
    DMA(rd, bd, v1, 16 * w * 1024 + ko, 1024, K1AUX);
    DMA(rx, bx, v0, 32 * w * 1024 + ko, 0, K1AUX);
    DMA(rx, bx, v1, 32 * w * 1024 + ko, 1024, K1AUX);
    DMA(rx, bx, v0, 32 * w * 1024 + ko + 14 * 1024, 2048, K1AUX);
    DMA(rx, bx, v1, 32 * w * 1024 + ko + 14 * 1024, 3072, K1AUX);
  };
  issue(w8 >> 2, w8 >> 2);
  if (tid < 4) ((LAS unsigned*)(L + SC_CTL))[tid] = 0u;
  STAMP(1);
  const int ks = xcd >> 1, mb = (xcd & 1) * 8 + (j >> 2), nb = j & 3;
  const u16* A = ((ks >> 1) ? ci : cr) + (size_t)mb * 128 * S_ + (ks & 1) * 1024;
  const auto ra = RSRC(A, 127 * 4096 + 2048);
  const auto rb = RSRC(ws + WS_AB + (size_t)(nb * 64 + ks * 16) * 16384, 16 * 16384);
  const int va0 = (32 * w + (l >> 3)) * 4096 + sz0, va1 = (32 * w + (l >> 3)) * 4096 + sz1;
  const int vb = w * 4096 + l * 16;
  const int rot2 = (4 * nb + 2 * (mb & 7)) & 15;
  auto stageA = [&](int t_, int buf) {
    const int t = (t_ + rot2) & 15;
    DMA(ra, L + LD_A + buf * 16384 + w * 4096, va0, t * 128, 0, AAUX);
    DMA(ra, L + LD_A + buf * 16384 + w * 4096, va1, t * 128 + 1 * (32768 - 1024), 1024, AAUX);
    DMA(ra, L + LD_A + buf * 16384 + w * 4096, va0, t * 128 + 2 * (32768 - 1024), 2048, AAUX);
    DMA(ra, L + LD_A + buf * 16384 + w * 4096, va1, t * 128 + 3 * (32768 - 1024), 3072, AAUX);
  };
  auto stageB = [&](int t_, int buf) {
    const int t = (t_ + rot2) & 15;
    DMA(rb, L + LD_B + buf * 16384 + w * 4096, vb, t * 16384, 0, BAUX);
    DMA(rb, L + LD_B + buf * 16384 + w * 4096, vb, t * 16384, 1024, BAUX);
    DMA(rb, L + LD_B + buf * 16384 + w * 4096, vb, t * 16384, 2048, BAUX);
    DMA(rb, L + LD_B + buf * 16384 + w * 4096, vb, t * 16384, 3072, BAUX);
  };
  const int i16 = l & 15, q4 = l >> 4;
  unsigned tag;
  u64 tag64;
  auto make_tag = [&]() {
    const unsigned e = __builtin_amdgcn_readfirstlane(epoch_v);
    tag = (e + 1u) * 0x9E3779B1u ^ 0x5BD1E995u;
    tag64 = ((u64)tag << 32) | (u64)(e * 0x85EBCA77u + 0x27D4EB2Fu);
  };

  if (w8 >= 4) {
    issue(2, 2);
    make_tag();
    f32x4 sdv = {};
    const bf16x8 ones = {0x3f80, 0x3f80, 0x3f80, 0x3f80, 0x3f80, 0x3f80, 0x3f80, 0x3f80};
    const int sdd = 16384 + (w * 16 + i16) * 128;
    const int ps0 = (q4 ^ (i16 >> 1)) * 16, ps1 = ((4 + q4) ^ (i16 >> 1)) * 16;
#pragma unroll
    for (int t = 0; t < 8; ++t) {
      if (t >= 1 && t < 6) WAITV(12); else if (t == 6) WAITV(10); else if (t == 7) WAITV(8);
      WAITL();
      BAR();
      const LAS unsigned char* bb = L + LD_P1 + (t & 3) * P1_BUF;
      const bf16x8 d0 = *(const LAS bf16x8*)(bb + sdd + ps0), d1 = *(const LAS bf16x8*)(bb + sdd + ps1);
      SB();
      if (t < 5) issue(t + 3, (t + 3) & 3);
      if (t == 5) stageA(0, 0);
      if (t == 6) stageA(1, 1);
      SB();
      sdv = MFMA16(d0, ones, sdv);
      sdv = MFMA16(d1, ones, sdv);
      SB();
    }
    if (i16 == 0) *(LAS f32x4*)(L + SC_SD + (w * 16 + q4 * 4) * 4) = sdv;
    WAITL();
    BAR();
    STAMP(2);
    stageA(2, 2);
    stageA(3, 3);
    if (w8 == 4) {
      GAS u64* fl = (GAS u64*)(ws + WS_FLAG + FL_AB) + ((ks * 16 + mb) * 4 + nb) * 64 + ((WGFLAG & 1) ? (l & 15) : l);
      for (unsigned spins = 0;; ++spins) {
        if (__all(__hip_atomic_load(fl, RLX_AGENT) == tag64) || spins > SPIN_CAP) { TMO(6); break; }
        __builtin_amdgcn_s_sleep(1);
      }
    }
    WAITV(0);
    BAR();
    STAMP(3);
    stageB(1, 1);
    BAR();
    stageB(2, 2); stageB(3, 3);
    WAITV(8); BAR(); stageA(4, 0); stageB(4, 0);
    WAITV(12); BAR(); stageA(5, 1); stageB(5, 1);
#pragma unroll
    for (int t = 2; t < 12; ++t) {
      WAITV(16);
      BAR();
      stageA(t + 4, t & 3); stageB(t + 4, t & 3);
    }
    WAITV(16); BAR(); WAITV(8); BAR(); WAITV(0); BAR();
    STAMP(5);
  } else {
    {
      f32x16 acc0 = {}, acc1 = {}, gram = {};
      f32x4 rsum = {};
      const short sv = (i16 == (q4 & 1)) ? (short)0x3f80 : (short)0;
      const bf16x8 selB = {sv, sv, sv, sv, sv, sv, sv, sv};
      bf16x8 fx[2][4], f0[2][4], f1[2][4];
      int pos[4];
#pragma unroll
      for (int kk = 0; kk < 4; ++kk) pos[kk] = r * 128 + (((2 * kk + h) ^ ((r >> 1) & 7)) * 16);
      auto ldf = [&](int t, int kk) {
        const LAS unsigned char* bb = L + LD_P1 + (t & 3) * P1_BUF;
        fx[t & 1][kk] = *(const LAS bf16x8*)(bb + w * 4096 + pos[kk]);
        f0[t & 1][kk] = *(const LAS bf16x8*)(bb + 16384 + pos[kk]);
        f1[t & 1][kk] = *(const LAS bf16x8*)(bb + 16384 + 4096 + pos[kk]);
      };
      make_tag();
      WAITV(0);
      BAR();
      STAMP(2);
#pragma unroll
      for (int kk = 0; kk < 4; ++kk) ldf(0, kk);
#pragma unroll
      for (int t = 0; t < 7; ++t) {
        const int p = t & 1;
        WAITL();
        BAR();
#pragma unroll
        for (int kk = 0; kk < 4; ++kk) {
          ldf(t + 1, kk);
          SB();
          gram = MFMA32(fx[p][kk], fx[p][kk], gram);
          rsum = MFMA16(fx[p][kk], selB, rsum);
          acc0 = MFMA32(fx[p][kk], f0[p][kk], acc0);
          acc1 = MFMA32(fx[p][kk], f1[p][kk], acc1);
          SB();
        }
      }
#pragma unroll
      for (int kk = 0; kk < 4; ++kk) { gram = MFMA32(fx[1][kk], fx[1][kk], gram); rsum = MFMA16(fx[1][kk], selB, rsum); }
      SB();
      acc0 = MFMA32(fx[1][0], f0[1][0], acc0); acc1 = MFMA32(fx[1][0], f1[1][0], acc1);
      acc0 = MFMA32(fx[1][1], f0[1][1], acc0); acc1 = MFMA32(fx[1][1], f1[1][1], acc1);
      SB();
      LAS float* st = (LAS float*)(L + SC_ST + w * 512);
      if (i16 < 2) *(LAS f32x4*)(st + i16 * 16 + q4 * 4) = rsum;
      float dg;
      {
        const unsigned m0 = 0u - (unsigned)(r & 1), m1 = 0u - (unsigned)((r >> 1) & 1), m3 = 0u - (unsigned)((r >> 3) & 1), m4 = 0u - (unsigned)(r >> 4);
        float e[4];
#pragma unroll
        for (int g = 0; g < 4; ++g) e[g] = bsel(m1, bsel(m0, gram[4 * g + 3], gram[4 * g + 2]), bsel(m0, gram[4 * g + 1], gram[4 * g]));
        dg = bsel(m4, bsel(m3, e[3], e[2]), bsel(m3, e[1], e[0]));
      }
      SB();
      acc0 = MFMA32(fx[1][2], f0[1][2], acc0); acc1 = MFMA32(fx[1][2], f1[1][2], acc1);
      SB();
      {
        const float mean = st[r] * (1.0f / D_);
        float rs = __builtin_amdgcn_rsqf(fmaxf(dg * (1.0f / D_) - mean * mean, 0.f) + 1e-5f);
        if (isB) rs = -rs;
        if (h == ((r >> 2) & 1)) *(LAS f32x2*)(st + 32 + 2 * r) = (f32x2){mean * rs, rs};
      }
      SB();
      acc0 = MFMA32(fx[1][3], f0[1][3], acc0); acc1 = MFMA32(fx[1][3], f1[1][3], acc1);
      SB();
      WAITL();
      BAR();
      STAMP(16);
      const LAS float* sdl = (const LAS float*)(L + SC_SD);
      const float cs0 = sdl[r], cs1 = sdl[32 + r];
      const LAS f32x4* st4 = (const LAS f32x4*)(st + 32);
      unsigned p0[8], p1[8];
#pragma unroll
      for (int g = 0; g < 4; ++g) {
        const f32x4 sa = st4[4 * g + 2 * h], sbv = st4[4 * g + 2 * h + 1];
        p0[2 * g] = pk_bf16(acc0[4 * g] * sa[1] - sa[0] * cs0, acc0[4 * g + 1] * sa[3] - sa[2] * cs0);
        p0[2 * g + 1] = pk_bf16(acc0[4 * g + 2] * sbv[1] - sbv[0] * cs0, acc0[4 * g + 3] * sbv[3] - sbv[2] * cs0);
        p1[2 * g] = pk_bf16(acc1[4 * g] * sa[1] - sa[0] * cs1, acc1[4 * g + 1] * sa[3] - sa[2] * cs1);
        p1[2 * g + 1] = pk_bf16(acc1[4 * g + 2] * sbv[1] - sbv[0] * cs1, acc1[4 * g + 3] * sbv[3] - sbv[2] * cs1);
      }
      const int kk0 = isB * 2048 + sb * 128 + w * 32;
      const int kb = kk0 >> 6, kst = (kk0 >> 4) & 3;
      const int nbp = e8 >> 1, nt0 = (e8 & 1) * 2;
      const auto rab = RSRC(ws + WS_AB + (size_t)(nbp * 64 + kb) * 16384, 16384);
#pragma unroll
      for (int s2 = 0; s2 < 2; ++s2) {
        {
          auto t0 = __builtin_amdgcn_permlane32_swap(p0[4 * s2], p0[4 * s2 + 2], false, false);
          auto t1 = __builtin_amdgcn_permlane32_swap(p0[4 * s2 + 1], p0[4 * s2 + 3], false, false);
          u32x4 o = {t0[0], t1[0], t0[1], t1[1]};
          __builtin_amdgcn_raw_buffer_store_b128(o, rab, l * 16, ((kst + s2) * 4 + nt0) * 1024, AUX_SC1);
        }
        {
          auto t0 = __builtin_amdgcn_permlane32_swap(p1[4 * s2], p1[4 * s2 + 2], false, false);
          auto t1 = __builtin_amdgcn_permlane32_swap(p1[4 * s2 + 1], p1[4 * s2 + 3], false, false);
          u32x4 o = {t0[0], t1[0], t0[1], t1[1]};
          __builtin_amdgcn_raw_buffer_store_b128(o, rab, l * 16, ((kst + s2) * 4 + nt0 + 1) * 1024, AUX_SC1);
        }
      }
      STAMP(17);
      WAITV(0);
      unsigned arr1 = 0;
      if (l == 0) arr1 = __hip_atomic_fetch_add((LAS unsigned*)(L + SC_CTL), 1u, __ATOMIC_RELAXED, __HIP_MEMORY_SCOPE_WORKGROUP);
      if (__builtin_amdgcn_readfirstlane(arr1) == 3u && l < 16)
        __hip_atomic_store((GAS u64*)(ws + WS_FLAG + FL_AB) + (((isB * 2 + (sb >> 3)) * 16 + l) * 4 + nbp) * 64 + (sb & 7) * 2 + (e8 & 1), tag64, RLX_AGENT);
      STAMP(18);
    }
    const int wm = w >> 1, wn = w & 1;
    f32x16 acc[2][2] = {};
    int aoff[4];
    const int sw = (r >> 1) & 7;
#pragma unroll
    for (int kk = 0; kk < 4; ++kk) aoff[kk] = LD_A + (wm * 64 + r) * 128 + (((2 * kk + h) ^ sw) * 16);
    const int boff = LD_B + wn * 2048 + l * 16;
    bf16x8 fa[2][2], fb[2][2];
    auto ldf = [&](int t, int kk, int p) {
      const int bo = (t & 3) * 16384;
      fb[p][0] = *(const LAS bf16x8*)(L + bo + boff + kk * 4096);
      fa[p][0] = *(const LAS bf16x8*)(L + bo + aoff[kk]);
      fa[p][1] = *(const LAS bf16x8*)(L + bo + aoff[kk] + 4096);
      fb[p][1] = *(const LAS bf16x8*)(L + bo + boff + kk * 4096 + 1024);
    };
    auto mma = [&](int p) {
      acc[0][0] = MFMA32(fb[p][0], fa[p][0], acc[0][0]);
      acc[1][0] = MFMA32(fb[p][0], fa[p][1], acc[1][0]);
      acc[0][1] = MFMA32(fb[p][1], fa[p][0], acc[0][1]);
      acc[1][1] = MFMA32(fb[p][1], fa[p][1], acc[1][1]);
      SB();
    };
    BAR();
    STAMP(3);
    stageB(0, 0);
    WAITV(0);
    BAR();
    STAMP(4);
    ldf(0, 0, 0);
#pragma unroll
    for (int t = 0; t < 16; ++t) {
      ldf(t, 1, 1); SB(); mma(0);
      ldf(t, 2, 0); SB(); mma(1);
      ldf(t, 3, 1); SB(); mma(0);
      if (t < 15) { WAITL(); BAR(); ldf(t + 1, 0, 0); SB(); }
      mma(1);
    }
    STAMP(5);
    const auto rp = RSRC(ws + WS_PART + (size_t)((ks * 16 + mb) * 4 + nb) * 32768, 32768);
#pragma unroll
    for (int nt = 0; nt < 2; ++nt)
#pragma unroll
      for (int mt = 0; mt < 2; ++mt)
#pragma unroll
        for (int s2 = 0; s2 < 2; ++s2) {
          const f32x16& a = acc[mt][nt];
          const unsigned p0 = pk_bf16(a[8 * s2], a[8 * s2 + 1]), p1 = pk_bf16(a[8 * s2 + 2], a[8 * s2 + 3]);
          const unsigned p2 = pk_bf16(a[8 * s2 + 4], a[8 * s2 + 5]), p3 = pk_bf16(a[8 * s2 + 6], a[8 * s2 + 7]);
          auto t0 = __builtin_amdgcn_permlane32_swap(p0, p2, false, false);
          auto t1 = __builtin_amdgcn_permlane32_swap(p1, p3, false, false);
          const u32x4 o = {t0[0], t1[0], t0[1], t1[1]};
          __builtin_amdgcn_raw_buffer_store_b128(o, rp, (((w * 2 + mt) * 2 + nt) * 2 + s2) * 1024 + l * 16, 0, AUX_SC1);
          SB();
        }
    STAMP(6);
    WAITV(0);
    unsigned arr2 = 0;
    if (l == 0) arr2 = __hip_atomic_fetch_add((LAS unsigned*)(L + SC_CTL) + 1 + wm, 1u, __ATOMIC_RELAXED, __HIP_MEMORY_SCOPE_WORKGROUP);
    if (__builtin_amdgcn_readfirstlane(arr2) == 1u && l < 4)
      __hip_atomic_store((GAS u64*)(ws + WS_FLAG + FL_ARR) + (mb * 8 + wm * 4 + l) * 32 + ks * 4 + nb, tag64, RLX_AGENT);
    STAMP(7);
  }
  GAS u64* cand = (GAS u64*)(ws + WS_FLAG + FL_CAND);
  const auto ro = RSRC(out, S_ * D_ * 2);
  const u64 thi = (u64)tag << 32;

  if ((j & 1) == 0) {
    const int q2 = ks * 2 + (nb >> 1), ci_ = mb * 8 + q2;
    const int r16 = l & 15, c = tid >> 4;
    const int row = ci_ * 16 + r16;
    BAR();
    u32x4 xv[2], wv[2];
#pragma unroll
    for (int u = 0; u < 2; ++u) {
      xv[u] = *(const u32x4*)(x + (size_t)row * D_ + c * 8 + u * 256);
      wv[u] = *(const u32x4*)(wr + c * 8 + u * 256);
    }
    if (w8 == 5) {
      GAS u64* arr = (GAS u64*)(ws + WS_FLAG + FL_ARR) + ci_ * 32 + ((WGFLAG & 2) ? (l & 15) : r);
      for (unsigned spins = 0;; ++spins) {
        if (__all(__hip_atomic_load(arr, RLX_AGENT) == tag64) || spins > SPIN_CAP) { TMO(0); break; }
        __builtin_amdgcn_s_sleep(1);
      }
    }
    const auto rp = RSRC(ws + WS_PART, 256 * 32768);
    const int poff = (mb * 4 + (c >> 4)) * 32768 +
                     (((((q2 >> 2) * 2 + ((c >> 3) & 1)) * 2 + ((q2 >> 1) & 1)) * 2 + ((c >> 2) & 1)) * 2 + ((c >> 1) & 1)) * 1024 +
                     ((c & 1) * 32 + (q2 & 1) * 16 + r16) * 16;
    SB();
    BAR();
    STAMP(8);
    u32x4 pv[2][4];
#pragma unroll
    for (int u = 0; u < 2; ++u)
#pragma unroll
      for (int k2 = 0; k2 < 4; ++k2) pv[u][k2] = __builtin_amdgcn_raw_buffer_load_b128(rp, poff, (k2 * 64 + u * 2) * 32768, AUX_SC1);
    float sw = 0.f, wf[16], hv[16];
#pragma unroll
    for (int u = 0; u < 2; ++u)
#pragma unroll
      for (int i = 0; i < 4; ++i) {
        wf[8 * u + 2 * i] = bf_lo(wv[u][i]); wf[8 * u + 2 * i + 1] = bf_hi(wv[u][i]);
        sw += wf[8 * u + 2 * i] + wf[8 * u + 2 * i + 1];
        hv[8 * u + 2 * i] = bf_lo(xv[u][i]) + ((bf_lo(pv[u][0][i]) + bf_lo(pv[u][1][i])) + (bf_lo(pv[u][2][i]) + bf_lo(pv[u][3][i])));
        hv[8 * u + 2 * i + 1] = bf_hi(xv[u][i]) + ((bf_hi(pv[u][0][i]) + bf_hi(pv[u][1][i])) + (bf_hi(pv[u][2][i]) + bf_hi(pv[u][3][i])));
      }
    float mu = 0.f, m2 = 0.f, hw = 0.f;
#pragma unroll
    for (int i = 0; i < 4; ++i) mu += (hv[4 * i] + hv[4 * i + 1]) + (hv[4 * i + 2] + hv[4 * i + 3]);
    mu *= 0.0625f;
    STAMP(9);
#pragma unroll
    for (int i = 0; i < 16; ++i) { const float d = hv[i] - mu; m2 = fmaf(d, d, m2); hw = fmaf(hv[i], wf[i], hw); }
    float hs = fold16(hw, sw);
    {
      auto t = __builtin_amdgcn_permlane16_swap(__float_as_uint(mu), __float_as_uint(mu), false, false);
      const float a = __uint_as_float(t[0]), bb = __uint_as_float(t[1]), d = a - bb;
      m2 = fold16(m2, m2) + d * d * 8.0f; mu = (a + bb) * 0.5f;
    }
    hs = fold32(hs, hs);
    {
      auto t = __builtin_amdgcn_permlane32_swap(__float_as_uint(mu), __float_as_uint(mu), false, false);
      const float a = __uint_as_float(t[0]), bb = __uint_as_float(t[1]), d = a - bb;
      m2 = fold32(m2, m2) + d * d * 16.0f; mu = (a + bb) * 0.5f;
    }
    LAS float* red = (LAS float*)(L + SC_RED);
    if (l < 16) *(LAS f32x2*)(red + (w8 * 16 + l) * 4) = (f32x2){mu, m2};
    if (l < 32) red[(w8 * 16 + r16) * 4 + 2 + (l >> 4)] = hs;
    WAITL();
    BAR();
    STAMP(10);
    float mean, M2, HW, SW;
    {
      const f32x4 ta = *(const LAS f32x4*)(red + l * 4), tb = *(const LAS f32x4*)(red + (64 + l) * 4);
      float ms = ta[0] + tb[0];
      float hs2 = fold16(ta[2] + tb[2], ta[3] + tb[3]);
      ms = fold16(ms, ms); ms = fold32(ms, ms);
      mean = ms * 0.125f;
      const float da = ta[0] - mean, db = tb[0] - mean;
      float q = (ta[1] + tb[1]) + 64.0f * (da * da + db * db);
      hs2 = fold32(hs2, hs2);
      q = fold16(q, q); M2 = fold32(q, q);
      auto t = __builtin_amdgcn_permlane16_swap(__float_as_uint(hs2), __float_as_uint(hs2), false, false);
      HW = __uint_as_float(t[0]); SW = __uint_as_float(t[1]);
    }
    const float rstd = __builtin_amdgcn_rsqf(M2 * (1.0f / D_) + 1e-5f);
    const float rr = (HW - mean * SW) * rstd;
    unsigned key = (ord_f32(rr) & ~15u) | (unsigned)(15 - r16);
    key = max(key, dppu<XOR1>(key)); key = max(key, dppu<XOR2>(key));
    key = max(key, dppu<XOR7>(key)); key = max(key, dppu<XOR8>(key));
    if (tid < 16) __hip_atomic_store(cand + tid * 128 + ci_, thi | (u64)key, RLX_AGENT);
    STAMP(11);
    const bool best = (15 - (int)(key & 15u)) == r16;
    float hn[16], ob[16];
#pragma unroll
    for (int i = 0; i < 16; ++i) { hn[i] = (hv[i] - mean) * rstd; ob[i] = hv[i] + hn[i]; }
    LAS unsigned* rowh = (LAS unsigned*)(L + SC_GU);
    LAS float* rowb = (LAS float*)(L + SC_X);
    if (best) {
#pragma unroll
      for (int u = 0; u < 2; ++u) {
        *(LAS u32x4*)(rowh + (c + 32 * u) * 4) = (u32x4){pk_bf16(hn[8 * u], hn[8 * u + 1]), pk_bf16(hn[8 * u + 2], hn[8 * u + 3]),
                                                         pk_bf16(hn[8 * u + 4], hn[8 * u + 5]), pk_bf16(hn[8 * u + 6], hn[8 * u + 7])};
        *(LAS f32x4*)(rowb + (c + 32 * u) * 8) = (f32x4){ob[8 * u], ob[8 * u + 1], ob[8 * u + 2], ob[8 * u + 3]};
        *(LAS f32x4*)(rowb + (c + 32 * u) * 8 + 4) = (f32x4){ob[8 * u + 4], ob[8 * u + 5], ob[8 * u + 6], ob[8 * u + 7]};
      }
    }
    WAITL();
    BAR();
    if (w8 < 4) __hip_atomic_store((GAS u64*)(ws + WS_FLAG + FL_HN + ci_ * 2048) + tid, thi | (u64)rowh[tid], RLX_AGENT);
    if (w8 == 7) {
      const f32x4 a = *(const LAS f32x4*)(rowb + l * 8), bq = *(const LAS f32x4*)(rowb + l * 8 + 4);
      const u32x4 o = {pk_bf16(a[0], a[1]), pk_bf16(a[2], a[3]), pk_bf16(bq[0], bq[1]), pk_bf16(bq[2], bq[3])};
      __builtin_amdgcn_raw_buffer_store_b128(o, ro, ((ci_ * 16 + 15 - (int)(key & 15u)) * D_ + l * 8) * 2, 0, AUX_SC1);
    }
    __hip_atomic_store((GAS u64*)(ws + WS_BASE + ci_ * 4096) + tid, thi | (u64)__float_as_uint(rowb[tid]), RLX_AGENT);
    if (w8 == 7) {
      WAITV(0);
      if (l == 0) __hip_atomic_store((GAS u64*)(ws + WS_FLAG + FL_DONE) + ci_ * 16, tag64, RLX_AGENT);
    }
    SB();
    if (!best) {
#pragma unroll
      for (int u = 0; u < 2; ++u) {
        const u32x4 o = {pk_bf16(ob[8 * u], ob[8 * u + 1]), pk_bf16(ob[8 * u + 2], ob[8 * u + 3]),
                         pk_bf16(ob[8 * u + 4], ob[8 * u + 5]), pk_bf16(ob[8 * u + 6], ob[8 * u + 7])};
        __builtin_amdgcn_raw_buffer_store_b128(o, ro, (row * D_ + (c + 32 * u) * 8) * 2, 0, OUTAUX);
      }
    }
    STAMP(12);
    TSDUMP(1, w8);
    return;
  }
  const int o2 = j >> 1;
  if (o2 < 7) {
    const int fp = xcd * 7 + o2;
    BAR();
    {
      const auto rg = RSRC(wg_, D_ * F_ * 2);
      const auto ru = RSRC(wu_, D_ * F_ * 2);
      const auto rw = RSRC(wd_ + (size_t)fp * 32 * D_, 32 * D_ * 2);
      const int vg = (w8 * 64 + (l >> 2)) * (F_ * 2) + fp * 64;
      const int cg = (l & 3) ^ (2 * (l >> 5));
#pragma unroll
      for (int i = 0; i < 4; ++i) {
        DMA(rg, L + (w8 * 4 + i) * 1024, vg + cg * 16, i * 16 * F_ * 2, 0, 0);
        DMA(ru, L + 32768 + (w8 * 4 + i) * 1024, vg + (cg ^ 1) * 16, i * 16 * F_ * 2, 0, 0);
      }
#pragma unroll
      for (int i = 0; i < 4; ++i) DMA(rw, L + 65536 + (w8 * 4 + i) * 1024, (l ^ (2 * i + 8 * ((w8 >> 1) & 1))) * 16, (w8 * 4 + i) * 1024, 0, 0);
    }
    STAMP(8);
    bf16x8 gf[16], df[4];
    WAITV(0);
    BAR();
    {
      const int q = (l & 15) >> 2, p = l & 3;
      if (w8 < 4) {
        const LAS unsigned char* pg = L + (p >> 1) * 32768 + (8 * q4 + q) * 64 + ((w ^ (2 * (q4 & 1)) ^ (p >> 1)) * 16) + (p & 1) * 8;
#pragma unroll
        for (int k2 = 0; k2 < 16; ++k2) gf[k2] = tr_frag(pg + k2 * 2048, pg + k2 * 2048 + 256);
      } else {
#pragma unroll
        for (int k2 = 0; k2 < 16; ++k2) gf[k2] = (bf16x8){};
      }
      const LAS unsigned char* pd = L + 65536 + (8 * q4 + q) * 1024 + (p & 1) * 8;
#pragma unroll
      for (int t = 0; t < 4; ++t) {
        const int ch = ((w8 * 8 + t * 2 + (p >> 1)) ^ (2 * q + 8 * (q4 & 1))) * 16;
        df[t] = tr_frag(pd + ch, pd + ch + 4096);
      }
#pragma unroll
      for (int k2 = 0; k2 < 16; ++k2) asm volatile("" : "+v"(gf[k2]));
#pragma unroll
      for (int t = 0; t < 4; ++t) asm volatile("" : "+v"(df[t]));
    }
    STAMP(9);
    if (w8 == 4) {
      GAS u64* cp = cand + xcd * 128 + l;
      int lead = 0;
      for (unsigned spins = 0;; ++spins) {
        const u64 c0 = __hip_atomic_load(cp, RLX_AGENT), c1 = __hip_atomic_load(cp + 64, RLX_AGENT);
        unsigned key; int nin;
        best_candidate(c0, c1, tag, key, lead, nin);
        if (nin >= LIGHT_TH || spins > SPIN_CAP) break;
        __builtin_amdgcn_s_sleep(LIGHT_SL);
      }
      for (unsigned spins = 0;; ++spins) {
        const u64 c0 = __hip_atomic_load(cp, RLX_AGENT), c1 = __hip_atomic_load(cp + 64, RLX_AGENT);
        GAS u64* hp = (GAS u64*)(ws + WS_FLAG + FL_HN + lead * 2048) + l * 4;
        const u64 g0 = __hip_atomic_load(hp, RLX_AGENT), g1 = __hip_atomic_load(hp + 1, RLX_AGENT);
        const u64 g2 = __hip_atomic_load(hp + 2, RLX_AGENT), g3 = __hip_atomic_load(hp + 3, RLX_AGENT);
        unsigned key; int wc, nin;
        best_candidate(c0, c1, tag, key, wc, nin);
        *(LAS u32x4*)(L + SC_X + l * 16) = (u32x4){(unsigned)g0, (unsigned)g1, (unsigned)g2, (unsigned)g3};
        const bool ok = ((unsigned)(g0 >> 32) == tag) & ((unsigned)(g1 >> 32) == tag) & ((unsigned)(g2 >> 32) == tag) & ((unsigned)(g3 >> 32) == tag);
        if ((nin == 128 && wc == lead && __all(ok)) || spins > SPIN_CAP) { TMO(1); break; }
        lead = wc;
        if (nin < 128) __builtin_amdgcn_s_sleep(1);
      }
    }
    WAITL();
    BAR();
    STAMP(11);
    if (w8 < 4) {
      f32x4 gu = {};
#pragma unroll
      for (int k2 = 0; k2 < 16; ++k2) gu = MFMA16(*(const LAS bf16x8*)(L + SC_X + k2 * 64 + q4 * 16), gf[k2], gu);
      const float g = gu[0], u = dpp<XOR8>(g);
      const float act = g * __builtin_amdgcn_rcpf(1.0f + __expf(-g)) * u;
      const unsigned pr = pk_bf16(act, dpp<XOR1>(act));
      if ((l & 0x39) == 0) ((LAS unsigned*)(L + SC_ACT))[w * 4 + (l >> 1)] = pr;
    }
    WAITL();
    BAR();
    {
      const bf16x8 af = *(const LAS bf16x8*)(L + SC_ACT + q4 * 16);
      const f32x4 z = {};
      const f32x4 y0 = MFMA16(af, df[0], z), y1 = MFMA16(af, df[1], z), y2 = MFMA16(af, df[2], z), y3 = MFMA16(af, df[3], z);
      const unsigned m0 = 0u - (unsigned)(q4 & 1), m1 = 0u - (unsigned)(q4 >> 1);
      const float y = bsel(m1, bsel(m0, y3[0], y2[0]), bsel(m0, y1[0], y0[0]));
      const float yn = dpp<XOR1>(y);
      if ((l & 1) == 0) {
        const int cp = tid >> 1;
        __hip_atomic_store((GAS u64*)(ws + WS_FLAG + FL_Y2 + ((cp >> 3) * 56 + fp) * 64) + (cp & 7), thi | (u64)pk_bf16(y, yn), RLX_AGENT);
      }
    }
    STAMP(12);
  } else if (o2 < 11 && w8 == 4) {
    const int cb = xcd * 4 + (o2 - 7);
    unsigned whi;
    u64 b0, b1;
    float y0, y1;
    int lead = 0;
    GAS u64* cp = cand + (8 + xcd) * 128 + l;
    for (int i = 0; i < FINAL_NAP; ++i) __builtin_amdgcn_s_sleep(64);
    for (unsigned spins = 0;; ++spins) {
      const u64 c0 = __hip_atomic_load(cp, RLX_AGENT), c1 = __hip_atomic_load(cp + 64, RLX_AGENT);
      int nin;
      best_candidate(c0, c1, tag, whi, lead, nin);
      if (nin == 128 || spins > SPIN_CAP) { TMO(5); break; }
      __builtin_amdgcn_s_sleep(4);
    }
    STAMP(10);
    const int wrow = lead * 16 + 15 - (int)(whi & 15u);
    const float sig = 1.0f / (1.0f + __expf(-unord_f32(whi)));
    for (unsigned spins = 0;; ++spins) {
      u64 g[7];
#pragma unroll
      for (int i = 0; i < 7; ++i) g[i] = __hip_atomic_load((GAS u64*)(ws + WS_FLAG + FL_Y2 + cb * 56 * 64) + l + i * 64, RLX_AGENT);
      const u64 dn = __hip_atomic_load((GAS u64*)(ws + WS_FLAG + FL_DONE) + lead * 16, RLX_AGENT);
      GAS u64* bp = (GAS u64*)(ws + WS_BASE + lead * 4096) + cb * 16 + (l & 7) * 2;
      b0 = __hip_atomic_load(bp, RLX_AGENT); b1 = __hip_atomic_load(bp + 1, RLX_AGENT);
      bool ok = (dn == tag64) & ((unsigned)(b0 >> 32) == tag) & ((unsigned)(b1 >> 32) == tag);
      y0 = 0.f; y1 = 0.f;
#pragma unroll
      for (int i = 0; i < 7; ++i) { ok &= (unsigned)(g[i] >> 32) == tag; y0 += bf_lo((unsigned)g[i]); y1 += bf_hi((unsigned)g[i]); }
      if (__all(ok) || spins > SPIN_CAP) { TMO(4); break; }
    }
    STAMP(13);
    y0 += dpp<XOR8>(y0); y1 += dpp<XOR8>(y1);
    float yy = fold16(y0, y1);
    yy = fold32(yy, yy);
    auto t = __builtin_amdgcn_permlane16_swap(__float_as_uint(yy), __float_as_uint(yy), false, false);
    const float f0 = __uint_as_float(t[0]), f1 = __uint_as_float(t[1]);
    if (l < 8)
      __builtin_amdgcn_raw_buffer_store_b32(pk_bf16(__uint_as_float((unsigned)b0) + f0 * sig, __uint_as_float((unsigned)b1) + f1 * sig),
                                            ro, (wrow * D_ + cb * 16 + l * 2) * 2, 0, AUX_SC1);
    if (cb == 0 && l == 0) __hip_atomic_store((GAS unsigned*)(ws + WS_FLAG + FL_EPOCH), epoch_v + 1u, RLX_AGENT);
    STAMP(14);
  }
  STAMP(15); TSDUMP(1, w8);
}

extern "C" void kernel_launch(void* const* d_in, const int* in_sizes, int n_in, void* d_out, int out_size, void* d_ws,
                              size_t ws_size, hipStream_t stream) {
  const u16* x = (const u16*)d_in[0];
  const u16* cr = (const u16*)d_in[1];
  const u16* ci = (const u16*)d_in[2];
  const u16* dr = (const u16*)d_in[3];
  const u16* di = (const u16*)d_in[4];
  const u16* wr = (const u16*)d_in[6];
  const u16* wg = (const u16*)d_in[7];
  const u16* wu = (const u16*)d_in[8];
  const u16* wd = (const u16*)d_in[9];
  (void)hipFuncSetAttribute((const void*)fused_kernel, hipFuncAttributeMaxDynamicSharedMemorySize, LDS_BYTES);
  fused_kernel<<<256, 512, LDS_BYTES, stream>>>(x, cr, ci, dr, di, wr, wg, wu, wd, (unsigned char*)d_ws, (u16*)d_out);
}
```

```cpp
#define WSPF 0
#define CODEPF 0
#include <hip/hip_runtime.h>
#include <stdint.h>

typedef unsigned short u16;
typedef short bf16x8 __attribute__((ext_vector_type(8)));
typedef __bf16 bf16x2_t __attribute__((ext_vector_type(2)));
typedef float f32x16 __attribute__((ext_vector_type(16)));
typedef float f32x4 __attribute__((ext_vector_type(4)));
typedef float f32x2 __attribute__((ext_vector_type(2)));
typedef unsigned u32x4 __attribute__((ext_vector_type(4)));
typedef unsigned u32x2 __attribute__((ext_vector_type(2)));
typedef unsigned long long u64;
#define DI __device__ __forceinline__
#define LAS __attribute__((address_space(3)))
#define GAS __attribute__((address_space(1)))
#define MFMA32(a, b, c) __builtin_amdgcn_mfma_f32_32x32x16_bf16((a), (b), (c), 0, 0, 0)
#define MFMA16(a, b, c) __builtin_amdgcn_mfma_f32_16x16x32_bf16((a), (b), (c), 0, 0, 0)
#define DMA(rs, ldsp, voff, soff, imm, aux) __builtin_amdgcn_raw_ptr_buffer_load_lds(rs, (LAS void*)(ldsp), 16, voff, soff, imm, aux)
#define WAITV(n) asm volatile("s_waitcnt vmcnt(" #n ")" ::: "memory")
#define WAITL() asm volatile("s_waitcnt lgkmcnt(0)" ::: "memory")
#define BAR() do { asm volatile("" ::: "memory"); __builtin_amdgcn_s_barrier(); asm volatile("" ::: "memory"); } while (0)
#define SB() __builtin_amdgcn_sched_barrier(0)
#define RLX_AGENT __ATOMIC_RELAXED, __HIP_MEMORY_SCOPE_AGENT
#define RSRC(p, bytes) __builtin_amdgcn_make_buffer_rsrc((void*)(p), (short)0, (int)(bytes), 0x00020000)
constexpr int AUX_NT = 2, AUX_SC1 = 16;
#define AAUX 0
#define BAUX AUX_SC1
#define OUTAUX AUX_SC1
#define FINAL_NAP 1
#define LIGHT_TH 64
#define LIGHT_SL 8
#define WGFLAG 3
#define SPEC_TH 96
#define K1AUX 0

#define CODE_BYTES 20480
constexpr int S_ = 2048, D_ = 512, F_ = 1792;
#define TSDECL
#define STAMP(i)
#define TSDUMP(kern, wave)

DI float bf_lo(unsigned w) { return __uint_as_float(w << 16); }
DI float bf_hi(unsigned w) { return __uint_as_float(w & 0xffff0000u); }
DI unsigned pk_bf16(float lo, float hi) {
  f32x2 v = {lo, hi};
  bf16x2_t b = __builtin_convertvector(v, bf16x2_t);
  return __builtin_bit_cast(unsigned, b);
}
DI float dot2(unsigned a, unsigned b, float acc) {
  return __builtin_amdgcn_fdot2_f32_bf16(__builtin_bit_cast(bf16x2_t, a), __builtin_bit_cast(bf16x2_t, b), acc, false);
}
template <int CTRL> DI float dpp(float x) {
  return __builtin_bit_cast(float, __builtin_amdgcn_mov_dpp(__builtin_bit_cast(int, x), CTRL, 0xf, 0xf, true));
}
template <int CTRL> DI unsigned dppu(unsigned x) { return (unsigned)__builtin_amdgcn_mov_dpp((int)x, CTRL, 0xf, 0xf, true); }
constexpr int XOR1 = 0xB1, XOR2 = 0x4E, XOR7 = 0x141, XOR8 = 0x128;
DI float fold32(float a, float b) {
  auto t = __builtin_amdgcn_permlane32_swap(__float_as_uint(a), __float_as_uint(b), false, false);
  return __uint_as_float(t[0]) + __uint_as_float(t[1]);
}
DI float fold16(float a, float b) {
  auto t = __builtin_amdgcn_permlane16_swap(__float_as_uint(a), __float_as_uint(b), false, false);
  return __uint_as_float(t[0]) + __uint_as_float(t[1]);
}
DI float bsel(unsigned m, float a, float b) { return __uint_as_float((m & __float_as_uint(a)) | (~m & __float_as_uint(b))); }
DI unsigned ord_f32(float v) { const unsigned u = __float_as_uint(v); return u ^ ((unsigned)((int)u >> 31) | 0x80000000u); }
DI float unord_f32(unsigned o) { return __uint_as_float(o ^ ((o >> 31) ? 0x80000000u : 0xffffffffu)); }
DI void max2(unsigned& hi, unsigned& lo, unsigned ohi, unsigned olo) {
  const bool t = (ohi > hi) | ((ohi == hi) & (olo > lo));
  hi = t ? ohi : hi; lo = t ? olo : lo;
}

DI void best_candidate(u64 c0, u64 c1, unsigned tag, unsigned& key_out, int& slot, int& nin) {
  const bool v0 = (unsigned)(c0 >> 32) == tag, v1 = (unsigned)(c1 >> 32) == tag;
  const unsigned k0 = v0 ? (unsigned)c0 : 0u, k1 = v1 ? (unsigned)c1 : 0u;
  unsigned m = max(k0, k1);
  m = max(m, dppu<XOR1>(m)); m = max(m, dppu<XOR2>(m)); m = max(m, dppu<XOR7>(m)); m = max(m, dppu<XOR8>(m));
  { auto t = __builtin_amdgcn_permlane16_swap(m, m, false, false); m = max(t[0], t[1]); }
  { auto t = __builtin_amdgcn_permlane32_swap(m, m, false, false); m = max(t[0], t[1]); }
  const u64 e0 = __ballot(k0 == m), e1 = __ballot(k1 == m);
  slot = e0 ? __builtin_ctzll(e0) : 64 + __builtin_ctzll(e1);
  key_out = __builtin_amdgcn_readfirstlane(m);
  nin = __builtin_popcountll(__ballot(v0)) + __builtin_popcountll(__ballot(v1));
}
typedef short s16x4 __attribute__((ext_vector_type(4)));
DI bf16x8 tr_frag(const LAS unsigned char* p0, const LAS unsigned char* p1) {
  const s16x4 a = __builtin_amdgcn_ds_read_tr16_b64_v4i16((LAS s16x4*)p0), b = __builtin_amdgcn_ds_read_tr16_b64_v4i16((LAS s16x4*)p1);
  return __builtin_shufflevector(a, b, 0, 1, 2, 3, 4, 5, 6, 7);
}
constexpr size_t WS_AB = 0;
constexpr size_t WS_PART = WS_AB + (size_t)4096 * 512 * 2;
constexpr size_t WS_BASE = WS_PART + (size_t)256 * 32768;
constexpr size_t WS_FLAG = WS_BASE + (size_t)128 * 4096;
constexpr int FL_HN = 0;
constexpr int FL_Y2 = 128 * 2048;
constexpr int FL_AB = FL_Y2 + 32 * 56 * 64;
constexpr int FL_ARR = FL_AB + 256 * 512;
constexpr int FL_CAND = FL_ARR + 128 * 256;
constexpr int FL_DONE = FL_CAND + 16 * 1024;
constexpr int FL_EPOCH = FL_DONE + 128 * 128;
constexpr unsigned SPIN_CAP = 1u << 18;
#define TMO(code)

constexpr int LD_A = 0;
constexpr int LD_B = 65536;
constexpr int LD_P1 = 49152;
constexpr int P1_BUF = 24576;
constexpr int SC_ST = 147456;
constexpr int SC_SD = SC_ST + 2048;
constexpr int SC_PF = SC_SD + 256;
constexpr int SC_RED = SC_PF + 1024;
constexpr int SC_X = SC_RED + 2048;
constexpr int SC_GU = SC_X + 2048;
constexpr int SC_ACT = SC_GU + 2048;
constexpr int SC_CTL = SC_ACT + 1024;
constexpr int LDS_BYTES = SC_CTL + 64;
static_assert(LDS_BYTES <= 163840, "LDS");

__global__ __launch_bounds__(512, 2) void fused_kernel(const u16* __restrict__ x, const u16* __restrict__ cr,
                                                       const u16* __restrict__ ci, const u16* __restrict__ dr,
                                                       const u16* __restrict__ di, const u16* __restrict__ wr,
                                                       const u16* __restrict__ wg_, const u16* __restrict__ wu_,
                                                       const u16* __restrict__ wd_, unsigned char* ws, u16* out) {
  extern __shared__ __attribute__((aligned(16))) unsigned char smem[];
  LAS unsigned char* L = (LAS unsigned char*)smem;
  const int tid = threadIdx.x, w8 = __builtin_amdgcn_readfirstlane(tid >> 6), w = w8 & 3, l = tid & 63, r = l & 31, h = l >> 5;
  const int b = blockIdx.x, xcd = b & 7, j = b >> 3;
  TSDECL STAMP(0);
  const unsigned epoch_v = __hip_atomic_load((GAS unsigned*)(ws + WS_FLAG + FL_EPOCH), RLX_AGENT);

  const int isB = xcd >> 2, sb = ((xcd >> 1) & 1) * 8 + (xcd & 1) * 4 + (j & 3), e8 = j >> 2;
  const u16* dm = (isB ? di : dr) + (size_t)e8 * 64 * D_;
  const auto rx = RSRC(x + (size_t)sb * 128 * D_, 128 * 1024);
  const auto rd = RSRC(dm, 64 * 1024);
  const int sz0 = (((l & 7) ^ ((l >> 4) & 7)) * 16), sz1 = (((l & 7) ^ ((4 + (l >> 4)) & 7)) * 16);
  const int v0 = (l >> 3) * 1024 + sz0, v1 = (l >> 3) * 1024 + sz1 + 7 * 1024;
  const int rot1 = (e8 + 2 * (j & 3)) & 7;
  auto issue = [&](int t_, int buf) {
    const int ko = ((t_ + rot1) & 7) * 128;
    LAS unsigned char* bx = L + LD_P1 + buf * P1_BUF + w * 4096;
    LAS unsigned char* bd = L + LD_P1 + buf * P1_BUF + 16384 + w * 2048;
    DMA(rd, bd, v0, 16 * w * 1024 + ko, 0, K1AUX);
    DMA(rd, bd, v1, 16 * w * 1024 + ko, 1024, K1AUX);
    DMA(rx, bx, v0, 32 * w * 1024 + ko, 0, K1AUX);
    DMA(rx, bx, v1, 32 * w * 1024 + ko, 1024, K1AUX);
    DMA(rx, bx, v0, 32 * w * 1024 + ko + 14 * 1024, 2048, K1AUX);
    DMA(rx, bx, v1, 32 * w * 1024 + ko + 14 * 1024, 3072, K1AUX);
  };
  issue(w8 >> 2, w8 >> 2);
  if (tid < 4) ((LAS unsigned*)(L + SC_CTL))[tid] = 0u;
  STAMP(1);
  const int ks = xcd >> 1, mb = (xcd & 1) * 8 + (j >> 2), nb = j & 3;
  const u16* A = ((ks >> 1) ? ci : cr) + (size_t)mb * 128 * S_ + (ks & 1) * 1024;
  const auto ra = RSRC(A, 127 * 4096 + 2048);
  const auto rb = RSRC(ws + WS_AB + (size_t)(nb * 64 + ks * 16) * 16384, 16 * 16384);
  const int va0 = (32 * w + (l >> 3)) * 4096 + sz0, va1 = (32 * w + (l >> 3)) * 4096 + sz1;
  const int vb = w * 4096 + l * 16;
  const int rot2 = (4 * nb + 2 * (mb & 7)) & 15;
  auto stageA = [&](int t_, int buf) {
    const int t = (t_ + rot2) & 15;
    DMA(ra, L + LD_A + buf * 16384 + w * 4096, va0, t * 128, 0, AAUX);
    DMA(ra, L + LD_A + buf * 16384 + w * 4096, va1, t * 128 + 1 * (32768 - 1024), 1024, AAUX);
    DMA(ra, L + LD_A + buf * 16384 + w * 4096, va0, t * 128 + 2 * (32768 - 1024), 2048, AAUX);
    DMA(ra, L + LD_A + buf * 16384 + w * 4096, va1, t * 128 + 3 * (32768 - 1024), 3072, AAUX);
  };
  auto stageB = [&](int t_, int buf) {
    const int t = (t_ + rot2) & 15;
    DMA(rb, L + LD_B + buf * 16384 + w * 4096, vb, t * 16384, 0, BAUX);
    DMA(rb, L + LD_B + buf * 16384 + w * 4096, vb, t * 16384, 1024, BAUX);
    DMA(rb, L + LD_B + buf * 16384 + w * 4096, vb, t * 16384, 2048, BAUX);
    DMA(rb, L + LD_B + buf * 16384 + w * 4096, vb, t * 16384, 3072, BAUX);
  };
  const int i16 = l & 15, q4 = l >> 4;
  unsigned tag;
  u64 tag64;
  auto make_tag = [&]() {
    const unsigned e = __builtin_amdgcn_readfirstlane(epoch_v);
    tag = (e + 1u) * 0x9E3779B1u ^ 0x5BD1E995u;
    tag64 = ((u64)tag << 32) | (u64)(e * 0x85EBCA77u + 0x27D4EB2Fu);
  };

  if (w8 >= 4) {
    issue(2, 2);
    make_tag();
    f32x4 sdv = {};
    const bf16x8 ones = {0x3f80, 0x3f80, 0x3f80, 0x3f80, 0x3f80, 0x3f80, 0x3f80, 0x3f80};
    const int sdd = 16384 + (w * 16 + i16) * 128;
    const int ps0 = (q4 ^ (i16 >> 1)) * 16, ps1 = ((4 + q4) ^ (i16 >> 1)) * 16;
#pragma unroll
    for (int t = 0; t < 8; ++t) {
      if (t >= 1 && t < 6) WAITV(12); else if (t == 6) WAITV(10); else if (t == 7) WAITV(8);
      WAITL();
      BAR();
      const LAS unsigned char* bb = L + LD_P1 + (t & 3) * P1_BUF;
      const bf16x8 d0 = *(const LAS bf16x8*)(bb + sdd + ps0), d1 = *(const LAS bf16x8*)(bb + sdd + ps1);
      SB();
      if (t < 5) issue(t + 3, (t + 3) & 3);
      if (t == 5) stageA(0, 0);
      if (t == 6) stageA(1, 1);
      SB();
      sdv = MFMA16(d0, ones, sdv);
      sdv = MFMA16(d1, ones, sdv);
      SB();
    }
    if (i16 == 0) *(LAS f32x4*)(L + SC_SD + (w * 16 + q4 * 4) * 4) = sdv;
    WAITL();
    BAR();
    STAMP(2);
    stageA(2, 2);
    stageA(3, 3);
    if (w8 == 4) {
      GAS u64* fl = (GAS u64*)(ws + WS_FLAG + FL_AB) + ((ks * 16 + mb) * 4 + nb) * 64 + ((WGFLAG & 1) ? (l & 15) : l);
      for (unsigned spins = 0;; ++spins) {
        if (__all(__hip_atomic_load(fl, RLX_AGENT) == tag64) || spins > SPIN_CAP) { TMO(6); break; }
        __builtin_amdgcn_s_sleep(1);
      }
    }
    WAITV(0);
    BAR();
    STAMP(3);
    stageB(1, 1);
    BAR();
    stageB(2, 2); stageB(3, 3);
    WAITV(8); BAR(); stageA(4, 0); stageB(4, 0);
    WAITV(12); BAR(); stageA(5, 1); stageB(5, 1);
    const unsigned char* wpf = w < 2 ? (const unsigned char*)(w ? wu_ : wg_) + (size_t)j * 16 * (F_ * 2) + ((xcd * 448) & ~127)
                                     : (const unsigned char*)(wd_ + (size_t)(xcd * 224 + j * 7) * D_) + (w & 1) * 3584;
    const unsigned wadv = w < 2 ? 2 * F_ * 2 : 448, wsec = w < 2 ? F_ * 2 : 0;
    unsigned sdummy = 0;
#pragma unroll
    for (int t = 2; t < 12; ++t) {
      WAITV(16);
      BAR();
      stageA(t + 4, t & 3); stageB(t + 4, t & 3);
    }
    WAITV(16); BAR(); WAITV(8); BAR(); WAITV(0); BAR();
    asm volatile("s_waitcnt lgkmcnt(0)" : "+s"(sdummy));
    STAMP(5);
  } else {
    {
      f32x16 acc0 = {}, acc1 = {}, gram = {};
      f32x4 rsum = {};
      const short sv = (i16 == (q4 & 1)) ? (short)0x3f80 : (short)0;
      const bf16x8 selB = {sv, sv, sv, sv, sv, sv, sv, sv};
      bf16x8 fx[2][4], f0[2][4], f1[2][4];
      int pos[4];
#pragma unroll
      for (int kk = 0; kk < 4; ++kk) pos[kk] = r * 128 + (((2 * kk + h) ^ ((r >> 1) & 7)) * 16);
      auto ldf = [&](int t, int kk) {
        const LAS unsigned char* bb = L + LD_P1 + (t & 3) * P1_BUF;
        fx[t & 1][kk] = *(const LAS bf16x8*)(bb + w * 4096 + pos[kk]);
        f0[t & 1][kk] = *(const LAS bf16x8*)(bb + 16384 + pos[kk]);
        f1[t & 1][kk] = *(const LAS bf16x8*)(bb + 16384 + 4096 + pos[kk]);
      };
      make_tag();
      WAITV(0);
      BAR();
      STAMP(2);
#pragma unroll
      for (int kk = 0; kk < 4; ++kk) ldf(0, kk);
#pragma unroll
      for (int t = 0; t < 7; ++t) {
        const int p = t & 1;
        WAITL();
        BAR();
#pragma unroll
        for (int kk = 0; kk < 4; ++kk) {
          ldf(t + 1, kk);
          SB();
          gram = MFMA32(fx[p][kk], fx[p][kk], gram);
          rsum = MFMA16(fx[p][kk], selB, rsum);
          acc0 = MFMA32(fx[p][kk], f0[p][kk], acc0);
          acc1 = MFMA32(fx[p][kk], f1[p][kk], acc1);
          SB();
        }
      }
#pragma unroll
      for (int kk = 0; kk < 4; ++kk) { gram = MFMA32(fx[1][kk], fx[1][kk], gram); rsum = MFMA16(fx[1][kk], selB, rsum); }
      SB();
      acc0 = MFMA32(fx[1][0], f0[1][0], acc0); acc1 = MFMA32(fx[1][0], f1[1][0], acc1);
      acc0 = MFMA32(fx[1][1], f0[1][1], acc0); acc1 = MFMA32(fx[1][1], f1[1][1], acc1);
      SB();
      LAS float* st = (LAS float*)(L + SC_ST + w * 512);
      if (i16 < 2) *(LAS f32x4*)(st + i16 * 16 + q4 * 4) = rsum;
      float dg;
      {
        const unsigned m0 = 0u - (unsigned)(r & 1), m1 = 0u - (unsigned)((r >> 1) & 1), m3 = 0u - (unsigned)((r >> 3) & 1), m4 = 0u - (unsigned)(r >> 4);
        float e[4];
#pragma unroll
        for (int g = 0; g < 4; ++g) e[g] = bsel(m1, bsel(m0, gram[4 * g + 3], gram[4 * g + 2]), bsel(m0, gram[4 * g + 1], gram[4 * g]));
        dg = bsel(m4, bsel(m3, e[3], e[2]), bsel(m3, e[1], e[0]));
      }
      SB();
      acc0 = MFMA32(fx[1][2], f0[1][2], acc0); acc1 = MFMA32(fx[1][2], f1[1][2], acc1);
      SB();
      {
        const float mean = st[r] * (1.0f / D_);
        float rs = __builtin_amdgcn_rsqf(fmaxf(dg * (1.0f / D_) - mean * mean, 0.f) + 1e-5f);
        if (isB) rs = -rs;
        if (h == ((r >> 2) & 1)) *(LAS f32x2*)(st + 32 + 2 * r) = (f32x2){mean * rs, rs};
      }
      SB();
      acc0 = MFMA32(fx[1][3], f0[1][3], acc0); acc1 = MFMA32(fx[1][3], f1[1][3], acc1);
      SB();
      WAITL();
      BAR();
      STAMP(16);
      const LAS float* sdl = (const LAS float*)(L + SC_SD);
      const float cs0 = sdl[r], cs1 = sdl[32 + r];
      const LAS f32x4* st4 = (const LAS f32x4*)(st + 32);
      unsigned p0[8], p1[8];
#pragma unroll
      for (int g = 0; g < 4; ++g) {
        const f32x4 sa = st4[4 * g + 2 * h], sbv = st4[4 * g + 2 * h + 1];
        p0[2 * g] = pk_bf16(acc0[4 * g] * sa[1] - sa[0] * cs0, acc0[4 * g + 1] * sa[3] - sa[2] * cs0);
        p0[2 * g + 1] = pk_bf16(acc0[4 * g + 2] * sbv[1] - sbv[0] * cs0, acc0[4 * g + 3] * sbv[3] - sbv[2] * cs0);
        p1[2 * g] = pk_bf16(acc1[4 * g] * sa[1] - sa[0] * cs1, acc1[4 * g + 1] * sa[3] - sa[2] * cs1);
        p1[2 * g + 1] = pk_bf16(acc1[4 * g + 2] * sbv[1] - sbv[0] * cs1, acc1[4 * g + 3] * sbv[3] - sbv[2] * cs1);
      }
      const int kk0 = isB * 2048 + sb * 128 + w * 32;
      const int kb = kk0 >> 6, kst = (kk0 >> 4) & 3;
      const int nbp = e8 >> 1, nt0 = (e8 & 1) * 2;
      const auto rab = RSRC(ws + WS_AB + (size_t)(nbp * 64 + kb) * 16384, 16384);
#pragma unroll
      for (int s2 = 0; s2 < 2; ++s2) {
        {
          auto t0 = __builtin_amdgcn_permlane32_swap(p0[4 * s2], p0[4 * s2 + 2], false, false);
          auto t1 = __builtin_amdgcn_permlane32_swap(p0[4 * s2 + 1], p0[4 * s2 + 3], false, false);
          u32x4 o = {t0[0], t1[0], t0[1], t1[1]};
          __builtin_amdgcn_raw_buffer_store_b128(o, rab, l * 16, ((kst + s2) * 4 + nt0) * 1024, AUX_SC1);
        }
        {
          auto t0 = __builtin_amdgcn_permlane32_swap(p1[4 * s2], p1[4 * s2 + 2], false, false);
          auto t1 = __builtin_amdgcn_permlane32_swap(p1[4 * s2 + 1], p1[4 * s2 + 3], false, false);
          u32x4 o = {t0[0], t1[0], t0[1], t1[1]};
          __builtin_amdgcn_raw_buffer_store_b128(o, rab, l * 16, ((kst + s2) * 4 + nt0 + 1) * 1024, AUX_SC1);
        }
      }
      STAMP(17);
      WAITV(0);
      unsigned arr1 = 0;
      if (l == 0) arr1 = __hip_atomic_fetch_add((LAS unsigned*)(L + SC_CTL), 1u, __ATOMIC_RELAXED, __HIP_MEMORY_SCOPE_WORKGROUP);
      if (__builtin_amdgcn_readfirstlane(arr1) == 3u && l < 16)
        __hip_atomic_store((GAS u64*)(ws + WS_FLAG + FL_AB) + (((isB * 2 + (sb >> 3)) * 16 + l) * 4 + nbp) * 64 + (sb & 7) * 2 + (e8 & 1), tag64, RLX_AGENT);
      STAMP(18);
    }
    const int wm = w >> 1, wn = w & 1;
    f32x16 acc[2][2] = {};
    int aoff[4];
    const int sw = (r >> 1) & 7;
#pragma unroll
    for (int kk = 0; kk < 4; ++kk) aoff[kk] = LD_A + (wm * 64 + r) * 128 + (((2 * kk + h) ^ sw) * 16);
    const int boff = LD_B + wn * 2048 + l * 16;
    bf16x8 fa[2][2], fb[2][2];
    auto ldf = [&](int t, int kk, int p) {
      const int bo = (t & 3) * 16384;
      fb[p][0] = *(const LAS bf16x8*)(L + bo + boff + kk * 4096);
      fa[p][0] = *(const LAS bf16x8*)(L + bo + aoff[kk]);
      fa[p][1] = *(const LAS bf16x8*)(L + bo + aoff[kk] + 4096);
      fb[p][1] = *(const LAS bf16x8*)(L + bo + boff + kk * 4096 + 1024);
    };
    auto mma = [&](int p) {
      acc[0][0] = MFMA32(fb[p][0], fa[p][0], acc[0][0]);
      acc[1][0] = MFMA32(fb[p][0], fa[p][1], acc[1][0]);
      acc[0][1] = MFMA32(fb[p][1], fa[p][0], acc[0][1]);
      acc[1][1] = MFMA32(fb[p][1], fa[p][1], acc[1][1]);
      SB();
    };
    BAR();
    STAMP(3);
    stageB(0, 0);
    WAITV(0);
    BAR();
    STAMP(4);
    ldf(0, 0, 0);
#pragma unroll
    for (int t = 0; t < 16; ++t) {
      ldf(t, 1, 1); SB(); mma(0);
      ldf(t, 2, 0); SB(); mma(1);
      ldf(t, 3, 1); SB(); mma(0);
      if (t < 15) { WAITL(); BAR(); ldf(t + 1, 0, 0); SB(); }
      mma(1);
    }
    STAMP(5);
    const auto rp = RSRC(ws + WS_PART + (size_t)((ks * 16 + mb) * 4 + nb) * 32768, 32768);
#pragma unroll
    for (int nt = 0; nt < 2; ++nt)
#pragma unroll
      for (int mt = 0; mt < 2; ++mt)
#pragma unroll
        for (int s2 = 0; s2 < 2; ++s2) {
          const f32x16& a = acc[mt][nt];
          const unsigned p0 = pk_bf16(a[8 * s2], a[8 * s2 + 1]), p1 = pk_bf16(a[8 * s2 + 2], a[8 * s2 + 3]);
          const unsigned p2 = pk_bf16(a[8 * s2 + 4], a[8 * s2 + 5]), p3 = pk_bf16(a[8 * s2 + 6], a[8 * s2 + 7]);
          auto t0 = __builtin_amdgcn_permlane32_swap(p0, p2, false, false);
          auto t1 = __builtin_amdgcn_permlane32_swap(p1, p3, false, false);
          const u32x4 o = {t0[0], t1[0], t0[1], t1[1]};
          __builtin_amdgcn_raw_buffer_store_b128(o, rp, (((w * 2 + mt) * 2 + nt) * 2 + s2) * 1024 + l * 16, 0, AUX_SC1);
          SB();
        }
    STAMP(6);
    WAITV(0);
    unsigned arr2 = 0;
    if (l == 0) arr2 = __hip_atomic_fetch_add((LAS unsigned*)(L + SC_CTL) + 1 + wm, 1u, __ATOMIC_RELAXED, __HIP_MEMORY_SCOPE_WORKGROUP);
    if (__builtin_amdgcn_readfirstlane(arr2) == 1u && l < 4)
      __hip_atomic_store((GAS u64*)(ws + WS_FLAG + FL_ARR) + (mb * 8 + wm * 4 + l) * 32 + ks * 4 + nb, tag64, RLX_AGENT);
    STAMP(7);
  }
  GAS u64* cand = (GAS u64*)(ws + WS_FLAG + FL_CAND);
  const auto ro = RSRC(out, S_ * D_ * 2);
  const u64 thi = (u64)tag << 32;

  if ((j & 1) == 0) {
    const int q2 = ks * 2 + (nb >> 1), ci_ = mb * 8 + q2;
    const int r16 = l & 15, c = tid >> 4;
    const int row = ci_ * 16 + r16;
    BAR();
    u32x4 xv[2], wv[2];
#pragma unroll
    for (int u = 0; u < 2; ++u) {
      xv[u] = *(const u32x4*)(x + (size_t)row * D_ + c * 8 + u * 256);
      wv[u] = *(const u32x4*)(wr + c * 8 + u * 256);
    }
    if (w8 == 5) {
      GAS u64* arr = (GAS u64*)(ws + WS_FLAG + FL_ARR) + ci_ * 32 + ((WGFLAG & 2) ? (l & 15) : r);
      for (unsigned spins = 0;; ++spins) {
        if (__all(__hip_atomic_load(arr, RLX_AGENT) == tag64) || spins > SPIN_CAP) { TMO(0); break; }
        __builtin_amdgcn_s_sleep(1);
      }
    }
    const auto rp = RSRC(ws + WS_PART, 256 * 32768);
    const int poff = (mb * 4 + (c >> 4)) * 32768 +
                     (((((q2 >> 2) * 2 + ((c >> 3) & 1)) * 2 + ((q2 >> 1) & 1)) * 2 + ((c >> 2) & 1)) * 2 + ((c >> 1) & 1)) * 1024 +
                     ((c & 1) * 32 + (q2 & 1) * 16 + r16) * 16;
    SB();
    BAR();
    STAMP(8);
    u32x4 pv[2][4];
#pragma unroll
    for (int u = 0; u < 2; ++u)
#pragma unroll
      for (int k2 = 0; k2 < 4; ++k2) pv[u][k2] = __builtin_amdgcn_raw_buffer_load_b128(rp, poff, (k2 * 64 + u * 2) * 32768, AUX_SC1);
    float sw = 0.f, wf[16], hv[16];
#pragma unroll
    for (int u = 0; u < 2; ++u)
#pragma unroll
      for (int i = 0; i < 4; ++i) {
        wf[8 * u + 2 * i] = bf_lo(wv[u][i]); wf[8 * u + 2 * i + 1] = bf_hi(wv[u][i]);
        sw += wf[8 * u + 2 * i] + wf[8 * u + 2 * i + 1];
        hv[8 * u + 2 * i] = bf_lo(xv[u][i]) + ((bf_lo(pv[u][0][i]) + bf_lo(pv[u][1][i])) + (bf_lo(pv[u][2][i]) + bf_lo(pv[u][3][i])));
        hv[8 * u + 2 * i + 1] = bf_hi(xv[u][i]) + ((bf_hi(pv[u][0][i]) + bf_hi(pv[u][1][i])) + (bf_hi(pv[u][2][i]) + bf_hi(pv[u][3][i])));
      }
    float mu = 0.f, m2 = 0.f, hw = 0.f;
#pragma unroll
    for (int i = 0; i < 4; ++i) mu += (hv[4 * i] + hv[4 * i + 1]) + (hv[4 * i + 2] + hv[4 * i + 3]);
    mu *= 0.0625f;
    STAMP(9);
#pragma unroll
    for (int i = 0; i < 16; ++i) { const float d = hv[i] - mu; m2 = fmaf(d, d, m2); hw = fmaf(hv[i], wf[i], hw); }
    float hs = fold16(hw, sw);
    {
      auto t = __builtin_amdgcn_permlane16_swap(__float_as_uint(mu), __float_as_uint(mu), false, false);
      const float a = __uint_as_float(t[0]), bb = __uint_as_float(t[1]), d = a - bb;
      m2 = fold16(m2, m2) + d * d * 8.0f; mu = (a + bb) * 0.5f;
    }
    hs = fold32(hs, hs);
    {
      auto t = __builtin_amdgcn_permlane32_swap(__float_as_uint(mu), __float_as_uint(mu), false, false);
      const float a = __uint_as_float(t[0]), bb = __uint_as_float(t[1]), d = a - bb;
      m2 = fold32(m2, m2) + d * d * 16.0f; mu = (a + bb) * 0.5f;
    }
    LAS float* red = (LAS float*)(L + SC_RED);
    if (l < 16) *(LAS f32x2*)(red + (w8 * 16 + l) * 4) = (f32x2){mu, m2};
    if (l < 32) red[(w8 * 16 + r16) * 4 + 2 + (l >> 4)] = hs;
    WAITL();
    BAR();
    STAMP(10);
    float mean, M2, HW, SW;
    {
      const f32x4 ta = *(const LAS f32x4*)(red + l * 4), tb = *(const LAS f32x4*)(red + (64 + l) * 4);
      float ms = ta[0] + tb[0];
      float hs2 = fold16(ta[2] + tb[2], ta[3] + tb[3]);
      ms = fold16(ms, ms); ms = fold32(ms, ms);
      mean = ms * 0.125f;
      const float da = ta[0] - mean, db = tb[0] - mean;
      float q = (ta[1] + tb[1]) + 64.0f * (da * da + db * db);
      hs2 = fold32(hs2, hs2);
      q = fold16(q, q); M2 = fold32(q, q);
      auto t = __builtin_amdgcn_permlane16_swap(__float_as_uint(hs2), __float_as_uint(hs2), false, false);
      HW = __uint_as_float(t[0]); SW = __uint_as_float(t[1]);
    }
    const float rstd = __builtin_amdgcn_rsqf(M2 * (1.0f / D_) + 1e-5f);
    const float rr = (HW - mean * SW) * rstd;
    unsigned key = (ord_f32(rr) & ~15u) | (unsigned)(15 - r16);
    key = max(key, dppu<XOR1>(key)); key = max(key, dppu<XOR2>(key));
    key = max(key, dppu<XOR7>(key)); key = max(key, dppu<XOR8>(key));
    if (tid < 16) __hip_atomic_store(cand + tid * 128 + ci_, thi | (u64)key, RLX_AGENT);
    STAMP(11);
    const bool best = (15 - (int)(key & 15u)) == r16;
    float hn[16], ob[16];
#pragma unroll
    for (int i = 0; i < 16; ++i) { hn[i] = (hv[i] - mean) * rstd; ob[i] = hv[i] + hn[i]; }
    LAS unsigned* rowh = (LAS unsigned*)(L + SC_GU);
    LAS float* rowb = (LAS float*)(L + SC_X);
    if (best) {
#pragma unroll
      for (int u = 0; u < 2; ++u) {
        *(LAS u32x4*)(rowh + (c + 32 * u) * 4) = (u32x4){pk_bf16(hn[8 * u], hn[8 * u + 1]), pk_bf16(hn[8 * u + 2], hn[8 * u + 3]),
                                                         pk_bf16(hn[8 * u + 4], hn[8 * u + 5]), pk_bf16(hn[8 * u + 6], hn[8 * u + 7])};
        *(LAS f32x4*)(rowb + (c + 32 * u) * 8) = (f32x4){ob[8 * u], ob[8 * u + 1], ob[8 * u + 2], ob[8 * u + 3]};
        *(LAS f32x4*)(rowb + (c + 32 * u) * 8 + 4) = (f32x4){ob[8 * u + 4], ob[8 * u + 5], ob[8 * u + 6], ob[8 * u + 7]};
      }
    }
    WAITL();
    BAR();
    if (w8 < 4) __hip_atomic_store((GAS u64*)(ws + WS_FLAG + FL_HN + ci_ * 2048) + tid, thi | (u64)rowh[tid], RLX_AGENT);
    if (w8 == 7) {
      const f32x4 a = *(const LAS f32x4*)(rowb + l * 8), bq = *(const LAS f32x4*)(rowb + l * 8 + 4);
      const u32x4 o = {pk_bf16(a[0], a[1]), pk_bf16(a[2], a[3]), pk_bf16(bq[0], bq[1]), pk_bf16(bq[2], bq[3])};
      __builtin_amdgcn_raw_buffer_store_b128(o, ro, ((ci_ * 16 + 15 - (int)(key & 15u)) * D_ + l * 8) * 2, 0, AUX_SC1);
    }
    __hip_atomic_store((GAS u64*)(ws + WS_BASE + ci_ * 4096) + tid, thi | (u64)__float_as_uint(rowb[tid]), RLX_AGENT);
    if (w8 == 7) {
      WAITV(0);
      if (l == 0) __hip_atomic_store((GAS u64*)(ws + WS_FLAG + FL_DONE) + ci_ * 16, tag64, RLX_AGENT);
    }
    SB();
    if (!best) {
#pragma unroll
      for (int u = 0; u < 2; ++u) {
        const u32x4 o = {pk_bf16(ob[8 * u], ob[8 * u + 1]), pk_bf16(ob[8 * u + 2], ob[8 * u + 3]),
                         pk_bf16(ob[8 * u + 4], ob[8 * u + 5]), pk_bf16(ob[8 * u + 6], ob[8 * u + 7])};
        __builtin_amdgcn_raw_buffer_store_b128(o, ro, (row * D_ + (c + 32 * u) * 8) * 2, 0, OUTAUX);
      }
    }
    STAMP(12);
    TSDUMP(1, w8);
    return;
  }
  const int o2 = j >> 1;
  if (o2 < 7) {
    const int fp = xcd * 7 + o2;
    BAR();
    {
      const auto rg = RSRC(wg_, D_ * F_ * 2);
      const auto ru = RSRC(wu_, D_ * F_ * 2);
      const auto rw = RSRC(wd_ + (size_t)fp * 32 * D_, 32 * D_ * 2);
      const int vg = (w8 * 64 + (l >> 2)) * (F_ * 2) + fp * 64;
      const int cg = (l & 3) ^ (2 * (l >> 5));
#pragma unroll
      for (int i = 0; i < 4; ++i) {
        DMA(rg, L + (w8 * 4 + i) * 1024, vg + cg * 16, i * 16 * F_ * 2, 0, 0);
        DMA(ru, L + 32768 + (w8 * 4 + i) * 1024, vg + (cg ^ 1) * 16, i * 16 * F_ * 2, 0, 0);
      }
#pragma unroll
      for (int i = 0; i < 4; ++i) DMA(rw, L + 65536 + (w8 * 4 + i) * 1024, (l ^ (2 * i + 8 * ((w8 >> 1) & 1))) * 16, (w8 * 4 + i) * 1024, 0, 0);
    }
    STAMP(8);
    bf16x8 gf[16], df[4];
    WAITV(0);
    BAR();
    {
      const int q = (l & 15) >> 2, p = l & 3;
      if (w8 < 4) {
        const LAS unsigned char* pg = L + (p >> 1) * 32768 + (8 * q4 + q) * 64 + ((w ^ (2 * (q4 & 1)) ^ (p >> 1)) * 16) + (p & 1) * 8;
#pragma unroll
        for (int k2 = 0; k2 < 16; ++k2) gf[k2] = tr_frag(pg + k2 * 2048, pg + k2 * 2048 + 256);
      } else {
#pragma unroll
        for (int k2 = 0; k2 < 16; ++k2) gf[k2] = (bf16x8){};
      }
      const LAS unsigned char* pd = L + 65536 + (8 * q4 + q) * 1024 + (p & 1) * 8;
#pragma unroll
      for (int t = 0; t < 4; ++t) {
        const int ch = ((w8 * 8 + t * 2 + (p >> 1)) ^ (2 * q + 8 * (q4 & 1))) * 16;
        df[t] = tr_frag(pd + ch, pd + ch + 4096);
      }
#pragma unroll
      for (int k2 = 0; k2 < 16; ++k2) asm volatile("" : "+v"(gf[k2]));
#pragma unroll
      for (int t = 0; t < 4; ++t) asm volatile("" : "+v"(df[t]));
    }
    STAMP(9);
    if (w8 == 4) {
      GAS u64* cp = cand + xcd * 128 + l;
      int lead = 0;
      for (unsigned spins = 0;; ++spins) {
        const u64 c0 = __hip_atomic_load(cp, RLX_AGENT), c1 = __hip_atomic_load(cp + 64, RLX_AGENT);
        unsigned key; int nin;
        best_candidate(c0, c1, tag, key, lead, nin);
        if (nin >= LIGHT_TH || spins > SPIN_CAP) break;
        __builtin_amdgcn_s_sleep(LIGHT_SL);
      }
      for (unsigned spins = 0;; ++spins) {
        const u64 c0 = __hip_atomic_load(cp, RLX_AGENT), c1 = __hip_atomic_load(cp + 64, RLX_AGENT);
        GAS u64* hp = (GAS u64*)(ws + WS_FLAG + FL_HN + lead * 2048) + l * 4;
        const u64 g0 = __hip_atomic_load(hp, RLX_AGENT), g1 = __hip_atomic_load(hp + 1, RLX_AGENT);
        const u64 g2 = __hip_atomic_load(hp + 2, RLX_AGENT), g3 = __hip_atomic_load(hp + 3, RLX_AGENT);
        unsigned key; int wc, nin;
        best_candidate(c0, c1, tag, key, wc, nin);
        *(LAS u32x4*)(L + SC_X + l * 16) = (u32x4){(unsigned)g0, (unsigned)g1, (unsigned)g2, (unsigned)g3};
        const bool ok = ((unsigned)(g0 >> 32) == tag) & ((unsigned)(g1 >> 32) == tag) & ((unsigned)(g2 >> 32) == tag) & ((unsigned)(g3 >> 32) == tag);
        if ((nin == 128 && wc == lead && __all(ok)) || spins > SPIN_CAP) { TMO(1); break; }
        lead = wc;
        if (nin < 128) __builtin_amdgcn_s_sleep(1);
      }
    }
    WAITL();
    BAR();
    STAMP(11);
    if (w8 < 4) {
      f32x4 gu = {};
#pragma unroll
      for (int k2 = 0; k2 < 16; ++k2) gu = MFMA16(*(const LAS bf16x8*)(L + SC_X + k2 * 64 + q4 * 16), gf[k2], gu);
      const float g = gu[0], u = dpp<XOR8>(g);
      const float act = g * __builtin_amdgcn_rcpf(1.0f + __expf(-g)) * u;
      const unsigned pr = pk_bf16(act, dpp<XOR1>(act));
      if ((l & 0x39) == 0) ((LAS unsigned*)(L + SC_ACT))[w * 4 + (l >> 1)] = pr;
    }
    WAITL();
    BAR();
    {
      const bf16x8 af = *(const LAS bf16x8*)(L + SC_ACT + q4 * 16);
      const f32x4 z = {};
      const f32x4 y0 = MFMA16(af, df[0], z), y1 = MFMA16(af, df[1], z), y2 = MFMA16(af, df[2], z), y3 = MFMA16(af, df[3], z);
      const unsigned m0 = 0u - (unsigned)(q4 & 1), m1 = 0u - (unsigned)(q4 >> 1);
      const float y = bsel(m1, bsel(m0, y3[0], y2[0]), bsel(m0, y1[0], y0[0]));
      const float yn = dpp<XOR1>(y);
      if ((l & 1) == 0) {
        const int cp = tid >> 1;
        __hip_atomic_store((GAS u64*)(ws + WS_FLAG + FL_Y2 + ((cp >> 3) * 56 + fp) * 64) + (cp & 7), thi | (u64)pk_bf16(y, yn), RLX_AGENT);
      }
    }
    STAMP(12);
  } else if (o2 < 11 && w8 == 4) {
    const int cb = xcd * 4 + (o2 - 7);
    unsigned whi;
    u64 b0, b1;
    float y0, y1;
    int lead = 0;
    GAS u64* cp = cand + (8 + xcd) * 128 + l;
    for (int i = 0; i < FINAL_NAP; ++i) __builtin_amdgcn_s_sleep(64);
    for (unsigned spins = 0;; ++spins) {
      const u64 c0 = __hip_atomic_load(cp, RLX_AGENT), c1 = __hip_atomic_load(cp + 64, RLX_AGENT);
      int nin;
      best_candidate(c0, c1, tag, whi, lead, nin);
      if (nin == 128 || spins > SPIN_CAP) { TMO(5); break; }
      __builtin_amdgcn_s_sleep(4);
    }
    STAMP(10);
    const int wrow = lead * 16 + 15 - (int)(whi & 15u);
    const float sig = 1.0f / (1.0f + __expf(-unord_f32(whi)));
    for (unsigned spins = 0;; ++spins) {
      u64 g[7];
#pragma unroll
      for (int i = 0; i < 7; ++i) g[i] = __hip_atomic_load((GAS u64*)(ws + WS_FLAG + FL_Y2 + cb * 56 * 64) + l + i * 64, RLX_AGENT);
      const u64 dn = __hip_atomic_load((GAS u64*)(ws + WS_FLAG + FL_DONE) + lead * 16, RLX_AGENT);
      GAS u64* bp = (GAS u64*)(ws + WS_BASE + lead * 4096) + cb * 16 + (l & 7) * 2;
      b0 = __hip_atomic_load(bp, RLX_AGENT); b1 = __hip_atomic_load(bp + 1, RLX_AGENT);
      bool ok = (dn == tag64) & ((unsigned)(b0 >> 32) == tag) & ((unsigned)(b1 >> 32) == tag);
      y0 = 0.f; y1 = 0.f;
#pragma unroll
      for (int i = 0; i < 7; ++i) { ok &= (unsigned)(g[i] >> 32) == tag; y0 += bf_lo((unsigned)g[i]); y1 += bf_hi((unsigned)g[i]); }
      if (__all(ok) || spins > SPIN_CAP) { TMO(4); break; }
    }
    STAMP(13);
    y0 += dpp<XOR8>(y0); y1 += dpp<XOR8>(y1);
    float yy = fold16(y0, y1);
    yy = fold32(yy, yy);
    auto t = __builtin_amdgcn_permlane16_swap(__float_as_uint(yy), __float_as_uint(yy), false, false);
    const float f0 = __uint_as_float(t[0]), f1 = __uint_as_float(t[1]);
    if (l < 8)
      __builtin_amdgcn_raw_buffer_store_b32(pk_bf16(__uint_as_float((unsigned)b0) + f0 * sig, __uint_as_float((unsigned)b1) + f1 * sig),
                                            ro, (wrow * D_ + cb * 16 + l * 2) * 2, 0, AUX_SC1);
    if (cb == 0 && l == 0) __hip_atomic_store((GAS unsigned*)(ws + WS_FLAG + FL_EPOCH), epoch_v + 1u, RLX_AGENT);
    STAMP(14);
  }
  STAMP(15); TSDUMP(1, w8);
}

extern "C" void kernel_launch(void* const* d_in, const int* in_sizes, int n_in, void* d_out, int out_size, void* d_ws,
                              size_t ws_size, hipStream_t stream) {
  const u16* x = (const u16*)d_in[0];
  const u16* cr = (const u16*)d_in[1];
  const u16* ci = (const u16*)d_in[2];
  const u16* dr = (const u16*)d_in[3];
  const u16* di = (const u16*)d_in[4];
  const u16* wr = (const u16*)d_in[6];
  const u16* wg = (const u16*)d_in[7];
  const u16* wu = (const u16*)d_in[8];
  const u16* wd = (const u16*)d_in[9];
  (void)hipFuncSetAttribute((const void*)fused_kernel, hipFuncAttributeMaxDynamicSharedMemorySize, LDS_BYTES);
  fused_kernel<<<256, 512, LDS_BYTES, stream>>>(x, cr, ci, dr, di, wr, wg, wu, wd, (unsigned char*)d_ws, (u16*)d_out);
}
```

```cpp
#include <hip/hip_runtime.h>
#include <stdint.h>

typedef unsigned short u16;
typedef short bf16x8 __attribute__((ext_vector_type(8)));
typedef __bf16 bf16x2_t __attribute__((ext_vector_type(2)));
typedef float f32x16 __attribute__((ext_vector_type(16)));
typedef float f32x4 __attribute__((ext_vector_type(4)));
typedef float f32x2 __attribute__((ext_vector_type(2)));
typedef unsigned u32x4 __attribute__((ext_vector_type(4)));
typedef unsigned u32x2 __attribute__((ext_vector_type(2)));
typedef unsigned long long u64;
#define DI __device__ __forceinline__
#define LAS __attribute__((address_space(3)))
#define GAS __attribute__((address_space(1)))
#define MFMA32(a, b, c) __builtin_amdgcn_mfma_f32_32x32x16_bf16((a), (b), (c), 0, 0, 0)
#define MFMA16(a, b, c) __builtin_amdgcn_mfma_f32_16x16x32_bf16((a), (b), (c), 0, 0, 0)
#define DMA(rs, ldsp, voff, soff, imm, aux) __builtin_amdgcn_raw_ptr_buffer_load_lds(rs, (LAS void*)(ldsp), 16, voff, soff, imm, aux)
#define WAITV(n) asm volatile("s_waitcnt vmcnt(" #n ")" ::: "memory")
#define WAITL() asm volatile("s_waitcnt lgkmcnt(0)" ::: "memory")
#define BAR() do { asm volatile("" ::: "memory"); __builtin_amdgcn_s_barrier(); asm volatile("" ::: "memory"); } while (0)
#define SB() __builtin_amdgcn_sched_barrier(0)
#define RLX_AGENT __ATOMIC_RELAXED, __HIP_MEMORY_SCOPE_AGENT
#define RSRC(p, bytes) __builtin_amdgcn_make_buffer_rsrc((void*)(p), (short)0, (int)(bytes), 0x00020000)
constexpr int AUX_NT = 2, AUX_SC1 = 16;
#define AAUX 0
#define BAUX AUX_SC1
#define OUTAUX AUX_SC1
#define FINAL_NAP 1
#define LIGHT_TH 64
#define LIGHT_SL 8
#define WGFLAG 3
#define AE 2
#define SPEC_TH 96
#define K1AUX 0

constexpr int S_ = 2048, D_ = 512, F_ = 1792;
#define TSDECL
#define STAMP(i)
#define TSDUMP(kern, wave)

DI float bf_lo(unsigned w) { return __uint_as_float(w << 16); }
DI float bf_hi(unsigned w) { return __uint_as_float(w & 0xffff0000u); }
DI unsigned pk_bf16(float lo, float hi) {
  f32x2 v = {lo, hi};
  bf16x2_t b = __builtin_convertvector(v, bf16x2_t);
  return __builtin_bit_cast(unsigned, b);
}
DI float dot2(unsigned a, unsigned b, float acc) {
  return __builtin_amdgcn_fdot2_f32_bf16(__builtin_bit_cast(bf16x2_t, a), __builtin_bit_cast(bf16x2_t, b), acc, false);
}
template <int CTRL> DI float dpp(float x) {
  return __builtin_bit_cast(float, __builtin_amdgcn_mov_dpp(__builtin_bit_cast(int, x), CTRL, 0xf, 0xf, true));
}
template <int CTRL> DI unsigned dppu(unsigned x) { return (unsigned)__builtin_amdgcn_mov_dpp((int)x, CTRL, 0xf, 0xf, true); }
constexpr int XOR1 = 0xB1, XOR2 = 0x4E, XOR7 = 0x141, XOR8 = 0x128;
DI float fold32(float a, float b) {
  auto t = __builtin_amdgcn_permlane32_swap(__float_as_uint(a), __float_as_uint(b), false, false);
  return __uint_as_float(t[0]) + __uint_as_float(t[1]);
}
DI float fold16(float a, float b) {
  auto t = __builtin_amdgcn_permlane16_swap(__float_as_uint(a), __float_as_uint(b), false, false);
  return __uint_as_float(t[0]) + __uint_as_float(t[1]);
}
DI float bsel(unsigned m, float a, float b) { return __uint_as_float((m & __float_as_uint(a)) | (~m & __float_as_uint(b))); }
DI unsigned ord_f32(float v) { const unsigned u = __float_as_uint(v); return u ^ ((unsigned)((int)u >> 31) | 0x80000000u); }
DI float unord_f32(unsigned o) { return __uint_as_float(o ^ ((o >> 31) ? 0x80000000u : 0xffffffffu)); }
DI void max2(unsigned& hi, unsigned& lo, unsigned ohi, unsigned olo) {
  const bool t = (ohi > hi) | ((ohi == hi) & (olo > lo));
  hi = t ? ohi : hi; lo = t ? olo : lo;
}

DI void best_candidate(u64 c0, u64 c1, unsigned tag, unsigned& key_out, int& slot, int& nin) {
  const bool v0 = (unsigned)(c0 >> 32) == tag, v1 = (unsigned)(c1 >> 32) == tag;
  const unsigned k0 = v0 ? (unsigned)c0 : 0u, k1 = v1 ? (unsigned)c1 : 0u;
  unsigned m = max(k0, k1);
  m = max(m, dppu<XOR1>(m)); m = max(m, dppu<XOR2>(m)); m = max(m, dppu<XOR7>(m)); m = max(m, dppu<XOR8>(m));
  { auto t = __builtin_amdgcn_permlane16_swap(m, m, false, false); m = max(t[0], t[1]); }
  { auto t = __builtin_amdgcn_permlane32_swap(m, m, false, false); m = max(t[0], t[1]); }
  const u64 e0 = __ballot(k0 == m), e1 = __ballot(k1 == m);
  slot = e0 ? __builtin_ctzll(e0) : 64 + __builtin_ctzll(e1);
  key_out = __builtin_amdgcn_readfirstlane(m);
  nin = __builtin_popcountll(__ballot(v0)) + __builtin_popcountll(__ballot(v1));
}
typedef short s16x4 __attribute__((ext_vector_type(4)));
DI bf16x8 tr_frag(const LAS unsigned char* p0, const LAS unsigned char* p1) {
  const s16x4 a = __builtin_amdgcn_ds_read_tr16_b64_v4i16((LAS s16x4*)p0), b = __builtin_amdgcn_ds_read_tr16_b64_v4i16((LAS s16x4*)p1);
  return __builtin_shufflevector(a, b, 0, 1, 2, 3, 4, 5, 6, 7);
}
constexpr int p1_extra(int b) { return (b >= AE && b < AE + 3) ? 4 : (b == AE + 3) ? 1 : 0; }
constexpr int p1_younger(int t) {
  int n = 0;
  if (t == 1) n += 6;
  for (int b = 0; b < t; ++b) {
    if (b + 3 > t && b < 5) n += 6;
    if (b + 3 >= t) n += p1_extra(b);
  }
  return n;
}
template <int N> DI void waitv_c() { asm volatile("s_waitcnt vmcnt(%0)" ::"n"(N) : "memory"); }
#define waitv_n(n) do { switch (t) { case 1: waitv_c<p1_younger(1)>(); break; case 2: waitv_c<p1_younger(2)>(); break; case 3: waitv_c<p1_younger(3)>(); break; \
    case 4: waitv_c<p1_younger(4)>(); break; case 5: waitv_c<p1_younger(5)>(); break; case 6: waitv_c<p1_younger(6)>(); break; default: waitv_c<p1_younger(7)>(); } } while (0)

constexpr size_t WS_AB = 0;
constexpr size_t WS_PART = WS_AB + (size_t)4096 * 512 * 2;
constexpr size_t WS_BASE = WS_PART + (size_t)256 * 32768;
constexpr size_t WS_FLAG = WS_BASE + (size_t)128 * 4096;
constexpr int FL_HN = 0;
constexpr int FL_Y2 = 128 * 2048;
constexpr int FL_AB = FL_Y2 + 32 * 56 * 64;
constexpr int FL_ARR = FL_AB + 256 * 512;
constexpr int FL_CAND = FL_ARR + 128 * 256;
constexpr int FL_DONE = FL_CAND + 16 * 1024;
constexpr int FL_EPOCH = FL_DONE + 128 * 128;
constexpr unsigned SPIN_CAP = 1u << 18;
#define TMO(code)

constexpr int LD_A = 0;
constexpr int LD_B = 65536;
constexpr int LD_P1 = 49152;
constexpr int P1_BUF = 24576;
constexpr int SC_ST = 147456;
constexpr int SC_SD = SC_ST + 2048;
constexpr int SC_PF = SC_SD + 256;
constexpr int SC_RED = SC_PF + 1024;
constexpr int SC_X = SC_RED + 2048;
constexpr int SC_GU = SC_X + 2048;
constexpr int SC_ACT = SC_GU + 2048;
constexpr int SC_CTL = SC_ACT + 1024;
constexpr int LDS_BYTES = SC_CTL + 64;
static_assert(LDS_BYTES <= 163840, "LDS");

__global__ __launch_bounds__(512, 2) void fused_kernel(const u16* __restrict__ x, const u16* __restrict__ cr,
                                                       const u16* __restrict__ ci, const u16* __restrict__ dr,
                                                       const u16* __restrict__ di, const u16* __restrict__ wr,
                                                       const u16* __restrict__ wg_, const u16* __restrict__ wu_,
                                                       const u16* __restrict__ wd_, unsigned char* ws, u16* out) {
  extern __shared__ __attribute__((aligned(16))) unsigned char smem[];
  LAS unsigned char* L = (LAS unsigned char*)smem;
  const int tid = threadIdx.x, w8 = __builtin_amdgcn_readfirstlane(tid >> 6), w = w8 & 3, l = tid & 63, r = l & 31, h = l >> 5;
  const int b = blockIdx.x, xcd = b & 7, j = b >> 3;
  TSDECL STAMP(0);
  const unsigned epoch_v = __hip_atomic_load((GAS unsigned*)(ws + WS_FLAG + FL_EPOCH), RLX_AGENT);

  const int isB = xcd >> 2, sb = ((xcd >> 1) & 1) * 8 + (xcd & 1) * 4 + (j & 3), e8 = j >> 2;
  const u16* dm = (isB ? di : dr) + (size_t)e8 * 64 * D_;
  const auto rx = RSRC(x + (size_t)sb * 128 * D_, 128 * 1024);
  const auto rd = RSRC(dm, 64 * 1024);
  const int sz0 = (((l & 7) ^ ((l >> 4) & 7)) * 16), sz1 = (((l & 7) ^ ((4 + (l >> 4)) & 7)) * 16);
  const int v0 = (l >> 3) * 1024 + sz0, v1 = (l >> 3) * 1024 + sz1 + 7 * 1024;
  const int rot1 = (e8 + 2 * (j & 3)) & 7;
  auto issue = [&](int t_, int buf) {
    const int ko = ((t_ + rot1) & 7) * 128;
    LAS unsigned char* bx = L + LD_P1 + buf * P1_BUF + w * 4096;
    LAS unsigned char* bd = L + LD_P1 + buf * P1_BUF + 16384 + w * 2048;
    DMA(rd, bd, v0, 16 * w * 1024 + ko, 0, K1AUX);
    DMA(rd, bd, v1, 16 * w * 1024 + ko, 1024, K1AUX);
    DMA(rx, bx, v0, 32 * w * 1024 + ko, 0, K1AUX);
    DMA(rx, bx, v1, 32 * w * 1024 + ko, 1024, K1AUX);
    DMA(rx, bx, v0, 32 * w * 1024 + ko + 14 * 1024, 2048, K1AUX);
    DMA(rx, bx, v1, 32 * w * 1024 + ko + 14 * 1024, 3072, K1AUX);
  };
  issue(w8 >> 2, w8 >> 2);
  if (tid < 4) ((LAS unsigned*)(L + SC_CTL))[tid] = 0u;
  STAMP(1);
  const int ks = xcd >> 1, mb = (xcd & 1) * 8 + (j >> 2), nb = j & 3;
  const u16* A = ((ks >> 1) ? ci : cr) + (size_t)mb * 128 * S_ + (ks & 1) * 1024;
  const auto ra = RSRC(A, 127 * 4096 + 2048);
  const auto rb = RSRC(ws + WS_AB + (size_t)(nb * 64 + ks * 16) * 16384, 16 * 16384);
  const int va0 = (32 * w + (l >> 3)) * 4096 + sz0, va1 = (32 * w + (l >> 3)) * 4096 + sz1;
  const int vb = w * 4096 + l * 16;
  const int rot2 = (4 * nb + 2 * (mb & 7)) & 15;
  auto stageA = [&](int t_, int buf) {
    const int t = (t_ + rot2) & 15;
    DMA(ra, L + LD_A + buf * 16384 + w * 4096, va0, t * 128, 0, AAUX);
    DMA(ra, L + LD_A + buf * 16384 + w * 4096, va1, t * 128 + 1 * (32768 - 1024), 1024, AAUX);
    DMA(ra, L + LD_A + buf * 16384 + w * 4096, va0, t * 128 + 2 * (32768 - 1024), 2048, AAUX);
    DMA(ra, L + LD_A + buf * 16384 + w * 4096, va1, t * 128 + 3 * (32768 - 1024), 3072, AAUX);
  };
  auto stageB = [&](int t_, int buf) {
    const int t = (t_ + rot2) & 15;
    DMA(rb, L + LD_B + buf * 16384 + w * 4096, vb, t * 16384, 0, BAUX);
    DMA(rb, L + LD_B + buf * 16384 + w * 4096, vb, t * 16384, 1024, BAUX);
    DMA(rb, L + LD_B + buf * 16384 + w * 4096, vb, t * 16384, 2048, BAUX);
    DMA(rb, L + LD_B + buf * 16384 + w * 4096, vb, t * 16384, 3072, BAUX);
  };
  const int i16 = l & 15, q4 = l >> 4;
  unsigned tag;
  u64 tag64;
  auto make_tag = [&]() {
    const unsigned e = __builtin_amdgcn_readfirstlane(epoch_v);
    tag = (e + 1u) * 0x9E3779B1u ^ 0x5BD1E995u;
    tag64 = ((u64)tag << 32) | (u64)(e * 0x85EBCA77u + 0x27D4EB2Fu);
  };

  if (w8 >= 4) {
    issue(2, 2);
    make_tag();
    f32x4 sdv = {};
    const bf16x8 ones = {0x3f80, 0x3f80, 0x3f80, 0x3f80, 0x3f80, 0x3f80, 0x3f80, 0x3f80};
    const int sdd = 16384 + (w * 16 + i16) * 128;
    const int ps0 = (q4 ^ (i16 >> 1)) * 16, ps1 = ((4 + q4) ^ (i16 >> 1)) * 16;
#pragma unroll
    for (int t = 0; t < 8; ++t) {
      if (t >= 1) waitv_n(p1_younger(t));
      WAITL();
      BAR();
      const LAS unsigned char* bb = L + LD_P1 + (t & 3) * P1_BUF;
      const bf16x8 d0 = *(const LAS bf16x8*)(bb + sdd + ps0), d1 = *(const LAS bf16x8*)(bb + sdd + ps1);
      SB();
      if (t < 5) issue(t + 3, (t + 3) & 3);
      if (t >= AE && t < AE + 3) stageA(t - AE, t - AE);
      if (t == AE + 3)
        __builtin_amdgcn_raw_ptr_buffer_load_lds(ra, (LAS void*)(L + SC_PF + w * 256), 4, l < 32 ? (32 * w + l) * 4096 : 0x7ffffff0, ((3 + rot2) & 15) * 128, 0, AAUX);
      SB();
      sdv = MFMA16(d0, ones, sdv);
      sdv = MFMA16(d1, ones, sdv);
      SB();
    }
    if (i16 == 0) *(LAS f32x4*)(L + SC_SD + (w * 16 + q4 * 4) * 4) = sdv;
    WAITL();
    BAR();
    STAMP(2);
    stageA(3, 3);
    if (w8 == 4) {
      GAS u64* fl = (GAS u64*)(ws + WS_FLAG + FL_AB) + ((ks * 16 + mb) * 4 + nb) * 64 + ((WGFLAG & 1) ? (l & 15) : l);
      for (unsigned spins = 0;; ++spins) {
        if (__all(__hip_atomic_load(fl, RLX_AGENT) == tag64) || spins > SPIN_CAP) { TMO(6); break; }
        __builtin_amdgcn_s_sleep(1);
      }
    }
    WAITV(0);
    BAR();
    STAMP(3);
    stageB(1, 1);
    BAR();
    stageB(2, 2); stageB(3, 3);
    WAITV(8); BAR(); stageA(4, 0); stageB(4, 0);
    WAITV(12); BAR(); stageA(5, 1); stageB(5, 1);
#pragma unroll
    for (int t = 2; t < 12; ++t) {
      WAITV(16);
      BAR();
      stageA(t + 4, t & 3); stageB(t + 4, t & 3);
    }
    WAITV(16); BAR(); WAITV(8); BAR(); WAITV(0); BAR();
    STAMP(5);
  } else {
    {
      f32x16 acc0 = {}, acc1 = {}, gram = {};
      f32x4 rsum = {};
      const short sv = (i16 == (q4 & 1)) ? (short)0x3f80 : (short)0;
      const bf16x8 selB = {sv, sv, sv, sv, sv, sv, sv, sv};
      bf16x8 fx[2][4], f0[2][4], f1[2][4];
      int pos[4];
#pragma unroll
      for (int kk = 0; kk < 4; ++kk) pos[kk] = r * 128 + (((2 * kk + h) ^ ((r >> 1) & 7)) * 16);
      auto ldf = [&](int t, int kk) {
        const LAS unsigned char* bb = L + LD_P1 + (t & 3) * P1_BUF;
        fx[t & 1][kk] = *(const LAS bf16x8*)(bb + w * 4096 + pos[kk]);
        f0[t & 1][kk] = *(const LAS bf16x8*)(bb + 16384 + pos[kk]);
        f1[t & 1][kk] = *(const LAS bf16x8*)(bb + 16384 + 4096 + pos[kk]);
      };
      make_tag();
      WAITV(0);
      BAR();
      STAMP(2);
#pragma unroll
      for (int kk = 0; kk < 4; ++kk) ldf(0, kk);
#pragma unroll
      for (int t = 0; t < 7; ++t) {
        const int p = t & 1;
        WAITL();
        BAR();
#pragma unroll
        for (int kk = 0; kk < 4; ++kk) {
          ldf(t + 1, kk);
          SB();
          gram = MFMA32(fx[p][kk], fx[p][kk], gram);
          rsum = MFMA16(fx[p][kk], selB, rsum);
          acc0 = MFMA32(fx[p][kk], f0[p][kk], acc0);
          acc1 = MFMA32(fx[p][kk], f1[p][kk], acc1);
          SB();
        }
      }
#pragma unroll
      for (int kk = 0; kk < 4; ++kk) { gram = MFMA32(fx[1][kk], fx[1][kk], gram); rsum = MFMA16(fx[1][kk], selB, rsum); }
      SB();
      acc0 = MFMA32(fx[1][0], f0[1][0], acc0); acc1 = MFMA32(fx[1][0], f1[1][0], acc1);
      acc0 = MFMA32(fx[1][1], f0[1][1], acc0); acc1 = MFMA32(fx[1][1], f1[1][1], acc1);
      SB();
      LAS float* st = (LAS float*)(L + SC_ST + w * 512);
      if (i16 < 2) *(LAS f32x4*)(st + i16 * 16 + q4 * 4) = rsum;
      float dg;
      {
        const unsigned m0 = 0u - (unsigned)(r & 1), m1 = 0u - (unsigned)((r >> 1) & 1), m3 = 0u - (unsigned)((r >> 3) & 1), m4 = 0u - (unsigned)(r >> 4);
        float e[4];
#pragma unroll
        for (int g = 0; g < 4; ++g) e[g] = bsel(m1, bsel(m0, gram[4 * g + 3], gram[4 * g + 2]), bsel(m0, gram[4 * g + 1], gram[4 * g]));
        dg = bsel(m4, bsel(m3, e[3], e[2]), bsel(m3, e[1], e[0]));
      }
      SB();
      acc0 = MFMA32(fx[1][2], f0[1][2], acc0); acc1 = MFMA32(fx[1][2], f1[1][2], acc1);
      SB();
      {
        const float mean = st[r] * (1.0f / D_);
        float rs = __builtin_amdgcn_rsqf(fmaxf(dg * (1.0f / D_) - mean * mean, 0.f) + 1e-5f);
        if (isB) rs = -rs;
        if (h == ((r >> 2) & 1)) *(LAS f32x2*)(st + 32 + 2 * r) = (f32x2){mean * rs, rs};
      }
      SB();
      acc0 = MFMA32(fx[1][3], f0[1][3], acc0); acc1 = MFMA32(fx[1][3], f1[1][3], acc1);
      SB();
      WAITL();
      BAR();
      STAMP(16);
      const LAS float* sdl = (const LAS float*)(L + SC_SD);
      const float cs0 = sdl[r], cs1 = sdl[32 + r];
      const LAS f32x4* st4 = (const LAS f32x4*)(st + 32);
      unsigned p0[8], p1[8];
#pragma unroll
      for (int g = 0; g < 4; ++g) {
        const f32x4 sa = st4[4 * g + 2 * h], sbv = st4[4 * g + 2 * h + 1];
        p0[2 * g] = pk_bf16(acc0[4 * g] * sa[1] - sa[0] * cs0, acc0[4 * g + 1] * sa[3] - sa[2] * cs0);
        p0[2 * g + 1] = pk_bf16(acc0[4 * g + 2] * sbv[1] - sbv[0] * cs0, acc0[4 * g + 3] * sbv[3] - sbv[2] * cs0);
        p1[2 * g] = pk_bf16(acc1[4 * g] * sa[1] - sa[0] * cs1, acc1[4 * g + 1] * sa[3] - sa[2] * cs1);
        p1[2 * g + 1] = pk_bf16(acc1[4 * g + 2] * sbv[1] - sbv[0] * cs1, acc1[4 * g + 3] * sbv[3] - sbv[2] * cs1);
      }
      const int kk0 = isB * 2048 + sb * 128 + w * 32;
      const int kb = kk0 >> 6, kst = (kk0 >> 4) & 3;
      const int nbp = e8 >> 1, nt0 = (e8 & 1) * 2;
      const auto rab = RSRC(ws + WS_AB + (size_t)(nbp * 64 + kb) * 16384, 16384);
#pragma unroll
      for (int s2 = 0; s2 < 2; ++s2) {
        {
          auto t0 = __builtin_amdgcn_permlane32_swap(p0[4 * s2], p0[4 * s2 + 2], false, false);
          auto t1 = __builtin_amdgcn_permlane32_swap(p0[4 * s2 + 1], p0[4 * s2 + 3], false, false);
          u32x4 o = {t0[0], t1[0], t0[1], t1[1]};
          __builtin_amdgcn_raw_buffer_store_b128(o, rab, l * 16, ((kst + s2) * 4 + nt0) * 1024, AUX_SC1);
        }
        {
          auto t0 = __builtin_amdgcn_permlane32_swap(p1[4 * s2], p1[4 * s2 + 2], false, false);
          auto t1 = __builtin_amdgcn_permlane32_swap(p1[4 * s2 + 1], p1[4 * s2 + 3], false, false);
          u32x4 o = {t0[0], t1[0], t0[1], t1[1]};
          __builtin_amdgcn_raw_buffer_store_b128(o, rab, l * 16, ((kst + s2) * 4 + nt0 + 1) * 1024, AUX_SC1);
        }
      }
      STAMP(17);
      WAITV(0);
      unsigned arr1 = 0;
      if (l == 0) arr1 = __hip_atomic_fetch_add((LAS unsigned*)(L + SC_CTL), 1u, __ATOMIC_RELAXED, __HIP_MEMORY_SCOPE_WORKGROUP);
      if (__builtin_amdgcn_readfirstlane(arr1) == 3u && l < 16)
        __hip_atomic_store((GAS u64*)(ws + WS_FLAG + FL_AB) + (((isB * 2 + (sb >> 3)) * 16 + l) * 4 + nbp) * 64 + (sb & 7) * 2 + (e8 & 1), tag64, RLX_AGENT);
      STAMP(18);
    }
    const int wm = w >> 1, wn = w & 1;
    f32x16 acc[2][2] = {};
    int aoff[4];
    const int sw = (r >> 1) & 7;
#pragma unroll
    for (int kk = 0; kk < 4; ++kk) aoff[kk] = LD_A + (wm * 64 + r) * 128 + (((2 * kk + h) ^ sw) * 16);
    const int boff = LD_B + wn * 2048 + l * 16;
    bf16x8 fa[2][2], fb[2][2];
    auto ldf = [&](int t, int kk, int p) {
      const int bo = (t & 3) * 16384;
      fb[p][0] = *(const LAS bf16x8*)(L + bo + boff + kk * 4096);
      fa[p][0] = *(const LAS bf16x8*)(L + bo + aoff[kk]);
      fa[p][1] = *(const LAS bf16x8*)(L + bo + aoff[kk] + 4096);
      fb[p][1] = *(const LAS bf16x8*)(L + bo + boff + kk * 4096 + 1024);
    };
    auto mma = [&](int p) {
      acc[0][0] = MFMA32(fb[p][0], fa[p][0], acc[0][0]);
      acc[1][0] = MFMA32(fb[p][0], fa[p][1], acc[1][0]);
      acc[0][1] = MFMA32(fb[p][1], fa[p][0], acc[0][1]);
      acc[1][1] = MFMA32(fb[p][1], fa[p][1], acc[1][1]);
      SB();
    };
    BAR();
    STAMP(3);
    stageB(0, 0);
    WAITV(0);
    BAR();
    STAMP(4);
    ldf(0, 0, 0);
#pragma unroll
    for (int t = 0; t < 16; ++t) {
      ldf(t, 1, 1); SB(); mma(0);
      ldf(t, 2, 0); SB(); mma(1);
      ldf(t, 3, 1); SB(); mma(0);
      if (t < 15) { WAITL(); BAR(); ldf(t + 1, 0, 0); SB(); }
      mma(1);
    }
    STAMP(5);
    const auto rp = RSRC(ws + WS_PART + (size_t)((ks * 16 + mb) * 4 + nb) * 32768, 32768);
#pragma unroll
    for (int nt = 0; nt < 2; ++nt)
#pragma unroll
      for (int mt = 0; mt < 2; ++mt)
#pragma unroll
        for (int s2 = 0; s2 < 2; ++s2) {
          const f32x16& a = acc[mt][nt];
          const unsigned p0 = pk_bf16(a[8 * s2], a[8 * s2 + 1]), p1 = pk_bf16(a[8 * s2 + 2], a[8 * s2 + 3]);
          const unsigned p2 = pk_bf16(a[8 * s2 + 4], a[8 * s2 + 5]), p3 = pk_bf16(a[8 * s2 + 6], a[8 * s2 + 7]);
          auto t0 = __builtin_amdgcn_permlane32_swap(p0, p2, false, false);
          auto t1 = __builtin_amdgcn_permlane32_swap(p1, p3, false, false);
          const u32x4 o = {t0[0], t1[0], t0[1], t1[1]};
          __builtin_amdgcn_raw_buffer_store_b128(o, rp, (((w * 2 + mt) * 2 + nt) * 2 + s2) * 1024 + l * 16, 0, AUX_SC1);
          SB();
        }
    STAMP(6);
    WAITV(0);
    unsigned arr2 = 0;
    if (l == 0) arr2 = __hip_atomic_fetch_add((LAS unsigned*)(L + SC_CTL) + 1 + wm, 1u, __ATOMIC_RELAXED, __HIP_MEMORY_SCOPE_WORKGROUP);
    if (__builtin_amdgcn_readfirstlane(arr2) == 1u && l < 4)
      __hip_atomic_store((GAS u64*)(ws + WS_FLAG + FL_ARR) + (mb * 8 + wm * 4 + l) * 32 + ks * 4 + nb, tag64, RLX_AGENT);
    STAMP(7);
  }
  GAS u64* cand = (GAS u64*)(ws + WS_FLAG + FL_CAND);
  const auto ro = RSRC(out, S_ * D_ * 2);
  const u64 thi = (u64)tag << 32;

  if ((j & 1) == 0) {
    const int q2 = ks * 2 + (nb >> 1), ci_ = mb * 8 + q2;
    const int r16 = l & 15, c = tid >> 4;
    const int row = ci_ * 16 + r16;
    BAR();
    u32x4 xv[2], wv[2];
#pragma unroll
    for (int u = 0; u < 2; ++u) {
      xv[u] = *(const u32x4*)(x + (size_t)row * D_ + c * 8 + u * 256);
      wv[u] = *(const u32x4*)(wr + c * 8 + u * 256);
    }
    if (w8 == 5) {
      GAS u64* arr = (GAS u64*)(ws + WS_FLAG + FL_ARR) + ci_ * 32 + ((WGFLAG & 2) ? (l & 15) : r);
      for (unsigned spins = 0;; ++spins) {
        if (__all(__hip_atomic_load(arr, RLX_AGENT) == tag64) || spins > SPIN_CAP) { TMO(0); break; }
        __builtin_amdgcn_s_sleep(1);
      }
    }
    const auto rp = RSRC(ws + WS_PART, 256 * 32768);
    const int poff = (mb * 4 + (c >> 4)) * 32768 +
                     (((((q2 >> 2) * 2 + ((c >> 3) & 1)) * 2 + ((q2 >> 1) & 1)) * 2 + ((c >> 2) & 1)) * 2 + ((c >> 1) & 1)) * 1024 +
                     ((c & 1) * 32 + (q2 & 1) * 16 + r16) * 16;
    SB();
    BAR();
    STAMP(8);
    u32x4 pv[2][4];
#pragma unroll
    for (int u = 0; u < 2; ++u)
#pragma unroll
      for (int k2 = 0; k2 < 4; ++k2) pv[u][k2] = __builtin_amdgcn_raw_buffer_load_b128(rp, poff, (k2 * 64 + u * 2) * 32768, AUX_SC1);
    float sw = 0.f, wf[16], hv[16];
#pragma unroll
    for (int u = 0; u < 2; ++u)
#pragma unroll
      for (int i = 0; i < 4; ++i) {
        wf[8 * u + 2 * i] = bf_lo(wv[u][i]); wf[8 * u + 2 * i + 1] = bf_hi(wv[u][i]);
        sw += wf[8 * u + 2 * i] + wf[8 * u + 2 * i + 1];
        hv[8 * u + 2 * i] = bf_lo(xv[u][i]) + ((bf_lo(pv[u][0][i]) + bf_lo(pv[u][1][i])) + (bf_lo(pv[u][2][i]) + bf_lo(pv[u][3][i])));
        hv[8 * u + 2 * i + 1] = bf_hi(xv[u][i]) + ((bf_hi(pv[u][0][i]) + bf_hi(pv[u][1][i])) + (bf_hi(pv[u][2][i]) + bf_hi(pv[u][3][i])));
      }
    float mu = 0.f, m2 = 0.f, hw = 0.f;
#pragma unroll
    for (int i = 0; i < 4; ++i) mu += (hv[4 * i] + hv[4 * i + 1]) + (hv[4 * i + 2] + hv[4 * i + 3]);
    mu *= 0.0625f;
    STAMP(9);
#pragma unroll
    for (int i = 0; i < 16; ++i) { const float d = hv[i] - mu; m2 = fmaf(d, d, m2); hw = fmaf(hv[i], wf[i], hw); }
    float hs = fold16(hw, sw);
    {
      auto t = __builtin_amdgcn_permlane16_swap(__float_as_uint(mu), __float_as_uint(mu), false, false);
      const float a = __uint_as_float(t[0]), bb = __uint_as_float(t[1]), d = a - bb;
      m2 = fold16(m2, m2) + d * d * 8.0f; mu = (a + bb) * 0.5f;
    }
    hs = fold32(hs, hs);
    {
      auto t = __builtin_amdgcn_permlane32_swap(__float_as_uint(mu), __float_as_uint(mu), false, false);
      const float a = __uint_as_float(t[0]), bb = __uint_as_float(t[1]), d = a - bb;
      m2 = fold32(m2, m2) + d * d * 16.0f; mu = (a + bb) * 0.5f;
    }
    LAS float* red = (LAS float*)(L + SC_RED);
    if (l < 16) *(LAS f32x2*)(red + (w8 * 16 + l) * 4) = (f32x2){mu, m2};
    if (l < 32) red[(w8 * 16 + r16) * 4 + 2 + (l >> 4)] = hs;
    WAITL();
    BAR();
    STAMP(10);
    float mean, M2, HW, SW;
    {
      const f32x4 ta = *(const LAS f32x4*)(red + l * 4), tb = *(const LAS f32x4*)(red + (64 + l) * 4);
      float ms = ta[0] + tb[0];
      float hs2 = fold16(ta[2] + tb[2], ta[3] + tb[3]);
      ms = fold16(ms, ms); ms = fold32(ms, ms);
      mean = ms * 0.125f;
      const float da = ta[0] - mean, db = tb[0] - mean;
      float q = (ta[1] + tb[1]) + 64.0f * (da * da + db * db);
      hs2 = fold32(hs2, hs2);
      q = fold16(q, q); M2 = fold32(q, q);
      auto t = __builtin_amdgcn_permlane16_swap(__float_as_uint(hs2), __float_as_uint(hs2), false, false);
      HW = __uint_as_float(t[0]); SW = __uint_as_float(t[1]);
    }
    const float rstd = __builtin_amdgcn_rsqf(M2 * (1.0f / D_) + 1e-5f);
    const float rr = (HW - mean * SW) * rstd;
    unsigned key = (ord_f32(rr) & ~15u) | (unsigned)(15 - r16);
    key = max(key, dppu<XOR1>(key)); key = max(key, dppu<XOR2>(key));
    key = max(key, dppu<XOR7>(key)); key = max(key, dppu<XOR8>(key));
    if (tid < 16) __hip_atomic_store(cand + tid * 128 + ci_, thi | (u64)key, RLX_AGENT);
    STAMP(11);
    const bool best = (15 - (int)(key & 15u)) == r16;
    float hn[16], ob[16];
#pragma unroll
    for (int i = 0; i < 16; ++i) { hn[i] = (hv[i] - mean) * rstd; ob[i] = hv[i] + hn[i]; }
    LAS unsigned* rowh = (LAS unsigned*)(L + SC_GU);
    LAS float* rowb = (LAS float*)(L + SC_X);
    if (best) {
#pragma unroll
      for (int u = 0; u < 2; ++u) {
        *(LAS u32x4*)(rowh + (c + 32 * u) * 4) = (u32x4){pk_bf16(hn[8 * u], hn[8 * u + 1]), pk_bf16(hn[8 * u + 2], hn[8 * u + 3]),
                                                         pk_bf16(hn[8 * u + 4], hn[8 * u + 5]), pk_bf16(hn[8 * u + 6], hn[8 * u + 7])};
        *(LAS f32x4*)(rowb + (c + 32 * u) * 8) = (f32x4){ob[8 * u], ob[8 * u + 1], ob[8 * u + 2], ob[8 * u + 3]};
        *(LAS f32x4*)(rowb + (c + 32 * u) * 8 + 4) = (f32x4){ob[8 * u + 4], ob[8 * u + 5], ob[8 * u + 6], ob[8 * u + 7]};
      }
    }
    WAITL();
    BAR();
    if (w8 < 4) __hip_atomic_store((GAS u64*)(ws + WS_FLAG + FL_HN + ci_ * 2048) + tid, thi | (u64)rowh[tid], RLX_AGENT);
    if (w8 == 7) {
      const f32x4 a = *(const LAS f32x4*)(rowb + l * 8), bq = *(const LAS f32x4*)(rowb + l * 8 + 4);
      const u32x4 o = {pk_bf16(a[0], a[1]), pk_bf16(a[2], a[3]), pk_bf16(bq[0], bq[1]), pk_bf16(bq[2], bq[3])};
      __builtin_amdgcn_raw_buffer_store_b128(o, ro, ((ci_ * 16 + 15 - (int)(key & 15u)) * D_ + l * 8) * 2, 0, AUX_SC1);
    }
    __hip_atomic_store((GAS u64*)(ws + WS_BASE + ci_ * 4096) + tid, thi | (u64)__float_as_uint(rowb[tid]), RLX_AGENT);
    if (w8 == 7) {
      WAITV(0);
      if (l == 0) __hip_atomic_store((GAS u64*)(ws + WS_FLAG + FL_DONE) + ci_ * 16, tag64, RLX_AGENT);
    }
    SB();
    if (!best) {
#pragma unroll
      for (int u = 0; u < 2; ++u) {
        const u32x4 o = {pk_bf16(ob[8 * u], ob[8 * u + 1]), pk_bf16(ob[8 * u + 2], ob[8 * u + 3]),
                         pk_bf16(ob[8 * u + 4], ob[8 * u + 5]), pk_bf16(ob[8 * u + 6], ob[8 * u + 7])};
        __builtin_amdgcn_raw_buffer_store_b128(o, ro, (row * D_ + (c + 32 * u) * 8) * 2, 0, OUTAUX);
      }
    }
    STAMP(12);
    TSDUMP(1, w8);
    return;
  }
  const int o2 = j >> 1;
  if (o2 < 7) {
    const int fp = xcd * 7 + o2;
    BAR();
    {
      const auto rg = RSRC(wg_, D_ * F_ * 2);
      const auto ru = RSRC(wu_, D_ * F_ * 2);
      const auto rw = RSRC(wd_ + (size_t)fp * 32 * D_, 32 * D_ * 2);
      const int vg = (w8 * 64 + (l >> 2)) * (F_ * 2) + fp * 64;
      const int cg = (l & 3) ^ (2 * (l >> 5));
#pragma unroll
      for (int i = 0; i < 4; ++i) {
        DMA(rg, L + (w8 * 4 + i) * 1024, vg + cg * 16, i * 16 * F_ * 2, 0, 0);
        DMA(ru, L + 32768 + (w8 * 4 + i) * 1024, vg + (cg ^ 1) * 16, i * 16 * F_ * 2, 0, 0);
      }
#pragma unroll
      for (int i = 0; i < 4; ++i) DMA(rw, L + 65536 + (w8 * 4 + i) * 1024, (l ^ (2 * i + 8 * ((w8 >> 1) & 1))) * 16, (w8 * 4 + i) * 1024, 0, 0);
    }
    STAMP(8);
    bf16x8 gf[16], df[4];
    WAITV(0);
    BAR();
    {
      const int q = (l & 15) >> 2, p = l & 3;
      if (w8 < 4) {
        const LAS unsigned char* pg = L + (p >> 1) * 32768 + (8 * q4 + q) * 64 + ((w ^ (2 * (q4 & 1)) ^ (p >> 1)) * 16) + (p & 1) * 8;
#pragma unroll
        for (int k2 = 0; k2 < 16; ++k2) gf[k2] = tr_frag(pg + k2 * 2048, pg + k2 * 2048 + 256);
      } else {
#pragma unroll
        for (int k2 = 0; k2 < 16; ++k2) gf[k2] = (bf16x8){};
      }
      const LAS unsigned char* pd = L + 65536 + (8 * q4 + q) * 1024 + (p & 1) * 8;
#pragma unroll
      for (int t = 0; t < 4; ++t) {
        const int ch = ((w8 * 8 + t * 2 + (p >> 1)) ^ (2 * q + 8 * (q4 & 1))) * 16;
        df[t] = tr_frag(pd + ch, pd + ch + 4096);
      }
#pragma unroll
      for (int k2 = 0; k2 < 16; ++k2) asm volatile("" : "+v"(gf[k2]));
#pragma unroll
      for (int t = 0; t < 4; ++t) asm volatile("" : "+v"(df[t]));
    }
    STAMP(9);
    if (w8 == 4) {
      GAS u64* cp = cand + xcd * 128 + l;
      int lead = 0;
      for (unsigned spins = 0;; ++spins) {
        const u64 c0 = __hip_atomic_load(cp, RLX_AGENT), c1 = __hip_atomic_load(cp + 64, RLX_AGENT);
        unsigned key; int nin;
        best_candidate(c0, c1, tag, key, lead, nin);
        if (nin >= LIGHT_TH || spins > SPIN_CAP) break;
        __builtin_amdgcn_s_sleep(LIGHT_SL);
      }
      for (unsigned spins = 0;; ++spins) {
        const u64 c0 = __hip_atomic_load(cp, RLX_AGENT), c1 = __hip_atomic_load(cp + 64, RLX_AGENT);
        GAS u64* hp = (GAS u64*)(ws + WS_FLAG + FL_HN + lead * 2048) + l * 4;
        const u64 g0 = __hip_atomic_load(hp, RLX_AGENT), g1 = __hip_atomic_load(hp + 1, RLX_AGENT);
        const u64 g2 = __hip_atomic_load(hp + 2, RLX_AGENT), g3 = __hip_atomic_load(hp + 3, RLX_AGENT);
        unsigned key; int wc, nin;
        best_candidate(c0, c1, tag, key, wc, nin);
        *(LAS u32x4*)(L + SC_X + l * 16) = (u32x4){(unsigned)g0, (unsigned)g1, (unsigned)g2, (unsigned)g3};
        const bool ok = ((unsigned)(g0 >> 32) == tag) & ((unsigned)(g1 >> 32) == tag) & ((unsigned)(g2 >> 32) == tag) & ((unsigned)(g3 >> 32) == tag);
        if ((nin == 128 && wc == lead && __all(ok)) || spins > SPIN_CAP) { TMO(1); break; }
        lead = wc;
        if (nin < 128) __builtin_amdgcn_s_sleep(1);
      }
    }
    WAITL();
    BAR();
    STAMP(11);
    if (w8 < 4) {
      f32x4 gu = {};
#pragma unroll
      for (int k2 = 0; k2 < 16; ++k2) gu = MFMA16(*(const LAS bf16x8*)(L + SC_X + k2 * 64 + q4 * 16), gf[k2], gu);
      const float g = gu[0], u = dpp<XOR8>(g);
      const float act = g * __builtin_amdgcn_rcpf(1.0f + __expf(-g)) * u;
      const unsigned pr = pk_bf16(act, dpp<XOR1>(act));
      if ((l & 0x39) == 0) ((LAS unsigned*)(L + SC_ACT))[w * 4 + (l >> 1)] = pr;
    }
    WAITL();
    BAR();
    {
      const bf16x8 af = *(const LAS bf16x8*)(L + SC_ACT + q4 * 16);
      const f32x4 z = {};
      const f32x4 y0 = MFMA16(af, df[0], z), y1 = MFMA16(af, df[1], z), y2 = MFMA16(af, df[2], z), y3 = MFMA16(af, df[3], z);
      const unsigned m0 = 0u - (unsigned)(q4 & 1), m1 = 0u - (unsigned)(q4 >> 1);
      const float y = bsel(m1, bsel(m0, y3[0], y2[0]), bsel(m0, y1[0], y0[0]));
      const float yn = dpp<XOR1>(y);
      if ((l & 1) == 0) {
        const int cp = tid >> 1;
        __hip_atomic_store((GAS u64*)(ws + WS_FLAG + FL_Y2 + ((cp >> 3) * 56 + fp) * 64) + (cp & 7), thi | (u64)pk_bf16(y, yn), RLX_AGENT);
      }
    }
    STAMP(12);
  } else if (o2 < 11 && w8 == 4) {
    const int cb = xcd * 4 + (o2 - 7);
    unsigned whi;
    u64 b0, b1;
    float y0, y1;
    int lead = 0;
    GAS u64* cp = cand + (8 + xcd) * 128 + l;
    for (int i = 0; i < FINAL_NAP; ++i) __builtin_amdgcn_s_sleep(64);
    for (unsigned spins = 0;; ++spins) {
      const u64 c0 = __hip_atomic_load(cp, RLX_AGENT), c1 = __hip_atomic_load(cp + 64, RLX_AGENT);
      int nin;
      best_candidate(c0, c1, tag, whi, lead, nin);
      if (nin == 128 || spins > SPIN_CAP) { TMO(5); break; }
      __builtin_amdgcn_s_sleep(4);
    }
    STAMP(10);
    const int wrow = lead * 16 + 15 - (int)(whi & 15u);
    const float sig = 1.0f / (1.0f + __expf(-unord_f32(whi)));
    for (unsigned spins = 0;; ++spins) {
      u64 g[7];
#pragma unroll
      for (int i = 0; i < 7; ++i) g[i] = __hip_atomic_load((GAS u64*)(ws + WS_FLAG + FL_Y2 + cb * 56 * 64) + l + i * 64, RLX_AGENT);
      const u64 dn = __hip_atomic_load((GAS u64*)(ws + WS_FLAG + FL_DONE) + lead * 16, RLX_AGENT);
      GAS u64* bp = (GAS u64*)(ws + WS_BASE + lead * 4096) + cb * 16 + (l & 7) * 2;
      b0 = __hip_atomic_load(bp, RLX_AGENT); b1 = __hip_atomic_load(bp + 1, RLX_AGENT);
      bool ok = (dn == tag64) & ((unsigned)(b0 >> 32) == tag) & ((unsigned)(b1 >> 32) == tag);
      y0 = 0.f; y1 = 0.f;
#pragma unroll
      for (int i = 0; i < 7; ++i) { ok &= (unsigned)(g[i] >> 32) == tag; y0 += bf_lo((unsigned)g[i]); y1 += bf_hi((unsigned)g[i]); }
      if (__all(ok) || spins > SPIN_CAP) { TMO(4); break; }
    }
    STAMP(13);
    y0 += dpp<XOR8>(y0); y1 += dpp<XOR8>(y1);
    float yy = fold16(y0, y1);
    yy = fold32(yy, yy);
    auto t = __builtin_amdgcn_permlane16_swap(__float_as_uint(yy), __float_as_uint(yy), false, false);
    const float f0 = __uint_as_float(t[0]), f1 = __uint_as_float(t[1]);
    if (l < 8)
      __builtin_amdgcn_raw_buffer_store_b32(pk_bf16(__uint_as_float((unsigned)b0) + f0 * sig, __uint_as_float((unsigned)b1) + f1 * sig),
                                            ro, (wrow * D_ + cb * 16 + l * 2) * 2, 0, AUX_SC1);
    if (cb == 0 && l == 0) __hip_atomic_store((GAS unsigned*)(ws + WS_FLAG + FL_EPOCH), epoch_v + 1u, RLX_AGENT);
    STAMP(14);
  }
  STAMP(15); TSDUMP(1, w8);
}

extern "C" void kernel_launch(void* const* d_in, const int* in_sizes, int n_in, void* d_out, int out_size, void* d_ws,
                              size_t ws_size, hipStream_t stream) {
  const u16* x = (const u16*)d_in[0];
  const u16* cr = (const u16*)d_in[1];
  const u16* ci = (const u16*)d_in[2];
  const u16* dr = (const u16*)d_in[3];
  const u16* di = (const u16*)d_in[4];
  const u16* wr = (const u16*)d_in[6];
  const u16* wg = (const u16*)d_in[7];
  const u16* wu = (const u16*)d_in[8];
  const u16* wd = (const u16*)d_in[9];
  (void)hipFuncSetAttribute((const void*)fused_kernel, hipFuncAttributeMaxDynamicSharedMemorySize, LDS_BYTES);
  fused_kernel<<<256, 512, LDS_BYTES, stream>>>(x, cr, ci, dr, di, wr, wg, wu, wd, (unsigned char*)d_ws, (u16*)d_out);
}
```

```cpp
#include <hip/hip_runtime.h>
#include <stdint.h>

typedef unsigned short u16;
typedef short bf16x8 __attribute__((ext_vector_type(8)));
typedef __bf16 bf16x2_t __attribute__((ext_vector_type(2)));
typedef float f32x16 __attribute__((ext_vector_type(16)));
typedef float f32x4 __attribute__((ext_vector_type(4)));
typedef float f32x2 __attribute__((ext_vector_type(2)));
typedef unsigned u32x4 __attribute__((ext_vector_type(4)));
typedef unsigned u32x2 __attribute__((ext_vector_type(2)));
typedef unsigned long long u64;
#define DI __device__ __forceinline__
#define LAS __attribute__((address_space(3)))
#define GAS __attribute__((address_space(1)))
#define MFMA32(a, b, c) __builtin_amdgcn_mfma_f32_32x32x16_bf16((a), (b), (c), 0, 0, 0)
#define MFMA16(a, b, c) __builtin_amdgcn_mfma_f32_16x16x32_bf16((a), (b), (c), 0, 0, 0)
#define DMA(rs, ldsp, voff, soff, imm, aux) __builtin_amdgcn_raw_ptr_buffer_load_lds(rs, (LAS void*)(ldsp), 16, voff, soff, imm, aux)
#define WAITV(n) asm volatile("s_waitcnt vmcnt(" #n ")" ::: "memory")
#define WAITL() asm volatile("s_waitcnt lgkmcnt(0)" ::: "memory")
#define BAR() do { asm volatile("" ::: "memory"); __builtin_amdgcn_s_barrier(); asm volatile("" ::: "memory"); } while (0)
#define SB() __builtin_amdgcn_sched_barrier(0)
#define RLX_AGENT __ATOMIC_RELAXED, __HIP_MEMORY_SCOPE_AGENT
#define RSRC(p, bytes) __builtin_amdgcn_make_buffer_rsrc((void*)(p), (short)0, (int)(bytes), 0x00020000)
constexpr int AUX_NT = 2, AUX_SC1 = 16;
#define AAUX 0
#define BAUX AUX_SC1
#define OUTAUX AUX_SC1
#define FINAL_NAP 1
#define LIGHT_TH 64
#define LIGHT_SL 8
#define WGFLAG 3
#define AE 1
#define STAT_T 5
#define ABNAP 0
#define ARNAP 0
#define HELP 1
#define WAUX 0
#define SPEC_TH 96
#define K1AUX 0

constexpr int S_ = 2048, D_ = 512, F_ = 1792;
#define TSDECL
#define STAMP(i)
#define TSDUMP(kern, wave)

DI float bf_lo(unsigned w) { return __uint_as_float(w << 16); }
DI float bf_hi(unsigned w) { return __uint_as_float(w & 0xffff0000u); }
DI unsigned pk_bf16(float lo, float hi) {
  f32x2 v = {lo, hi};
  bf16x2_t b = __builtin_convertvector(v, bf16x2_t);
  return __builtin_bit_cast(unsigned, b);
}
DI float dot2(unsigned a, unsigned b, float acc) {
  return __builtin_amdgcn_fdot2_f32_bf16(__builtin_bit_cast(bf16x2_t, a), __builtin_bit_cast(bf16x2_t, b), acc, false);
}
template <int CTRL> DI float dpp(float x) {
  return __builtin_bit_cast(float, __builtin_amdgcn_mov_dpp(__builtin_bit_cast(int, x), CTRL, 0xf, 0xf, true));
}
template <int CTRL> DI unsigned dppu(unsigned x) { return (unsigned)__builtin_amdgcn_mov_dpp((int)x, CTRL, 0xf, 0xf, true); }
constexpr int XOR1 = 0xB1, XOR2 = 0x4E, XOR7 = 0x141, XOR8 = 0x128;
DI float fold32(float a, float b) {
  auto t = __builtin_amdgcn_permlane32_swap(__float_as_uint(a), __float_as_uint(b), false, false);
  return __uint_as_float(t[0]) + __uint_as_float(t[1]);
}
DI float fold16(float a, float b) {
  auto t = __builtin_amdgcn_permlane16_swap(__float_as_uint(a), __float_as_uint(b), false, false);
  return __uint_as_float(t[0]) + __uint_as_float(t[1]);
}
DI float bsel(unsigned m, float a, float b) { return __uint_as_float((m & __float_as_uint(a)) | (~m & __float_as_uint(b))); }
DI unsigned ord_f32(float v) { const unsigned u = __float_as_uint(v); return u ^ ((unsigned)((int)u >> 31) | 0x80000000u); }
DI float unord_f32(unsigned o) { return __uint_as_float(o ^ ((o >> 31) ? 0x80000000u : 0xffffffffu)); }
DI void max2(unsigned& hi, unsigned& lo, unsigned ohi, unsigned olo) {
  const bool t = (ohi > hi) | ((ohi == hi) & (olo > lo));
  hi = t ? ohi : hi; lo = t ? olo : lo;
}

DI void best_candidate(u64 c0, u64 c1, unsigned tag, unsigned& key_out, int& slot, int& nin) {
  const bool v0 = (unsigned)(c0 >> 32) == tag, v1 = (unsigned)(c1 >> 32) == tag;
  const unsigned k0 = v0 ? (unsigned)c0 : 0u, k1 = v1 ? (unsigned)c1 : 0u;
  unsigned m = max(k0, k1);
  m = max(m, dppu<XOR1>(m)); m = max(m, dppu<XOR2>(m)); m = max(m, dppu<XOR7>(m)); m = max(m, dppu<XOR8>(m));
  { auto t = __builtin_amdgcn_permlane16_swap(m, m, false, false); m = max(t[0], t[1]); }
  { auto t = __builtin_amdgcn_permlane32_swap(m, m, false, false); m = max(t[0], t[1]); }
  const u64 e0 = __ballot(k0 == m), e1 = __ballot(k1 == m);
  slot = e0 ? __builtin_ctzll(e0) : 64 + __builtin_ctzll(e1);
  key_out = __builtin_amdgcn_readfirstlane(m);
  nin = __builtin_popcountll(__ballot(v0)) + __builtin_popcountll(__ballot(v1));
}
typedef short s16x4 __attribute__((ext_vector_type(4)));
DI bf16x8 tr_frag(const LAS unsigned char* p0, const LAS unsigned char* p1) {
  const s16x4 a = __builtin_amdgcn_ds_read_tr16_b64_v4i16((LAS s16x4*)p0), b = __builtin_amdgcn_ds_read_tr16_b64_v4i16((LAS s16x4*)p1);
  return __builtin_shufflevector(a, b, 0, 1, 2, 3, 4, 5, 6, 7);
}
constexpr int p1_extra(int b) { return ((b >= AE && b < AE + 3) ? 4 : (b == AE + 3) ? 1 : 0) + (b == 0 ? 1 : 0); }
constexpr int p1_younger(int t) {
  int n = 0;
  if (t == 1) n += 6;
  for (int b = 0; b < t; ++b) {
    if (b + 3 > t && b < 5) n += 6;
    if (b + 3 >= t) n += p1_extra(b);
  }
  return n;
}
template <int N> DI void waitv_c() { asm volatile("s_waitcnt vmcnt(%0)" ::"n"(N) : "memory"); }
#define waitv_n(n) do { switch (t) { case 1: waitv_c<p1_younger(1)>(); break; case 2: waitv_c<p1_younger(2)>(); break; case 3: waitv_c<p1_younger(3)>(); break; \
    case 4: waitv_c<p1_younger(4)>(); break; case 5: waitv_c<p1_younger(5)>(); break; case 6: waitv_c<p1_younger(6)>(); break; default: waitv_c<p1_younger(7)>(); } } while (0)

constexpr size_t WS_AB = 0;
constexpr size_t WS_PART = WS_AB + (size_t)4096 * 512 * 2;
constexpr size_t WS_BASE = WS_PART + (size_t)256 * 32768;
constexpr size_t WS_FLAG = WS_BASE + (size_t)128 * 4096;
constexpr int FL_HN = 0;
constexpr int FL_Y2 = 128 * 2048;
constexpr int FL_AB = FL_Y2 + 32 * 56 * 64;
constexpr int FL_ARR = FL_AB + 256 * 512;
constexpr int FL_CAND = FL_ARR + 128 * 256;
constexpr int FL_DONE = FL_CAND + 16 * 1024;
constexpr int FL_STAT = FL_DONE + 128 * 128;
constexpr int FL_EPOCH = FL_STAT + 2048 * 16;
constexpr unsigned SPIN_CAP = 1u << 18;
#define TMO(code)

constexpr int LD_A = 0;
constexpr int LD_B = 65536;
constexpr int LD_XR = 131072;
constexpr int LD_P1 = 49152;
constexpr int P1_BUF = 24576;
constexpr int SC_ST = 147456;
constexpr int SC_SD = SC_ST + 2048;
constexpr int SC_PF = SC_SD + 256;
constexpr int SC_RED = SC_PF + 1024;
constexpr int SC_X = SC_RED + 2048;
constexpr int SC_GU = SC_X + 2048;
constexpr int SC_ACT = SC_GU + 2048;
constexpr int SC_CTL = SC_ACT + 1024;
constexpr int LDS_BYTES = SC_CTL + 64;
static_assert(LDS_BYTES <= 163840, "LDS");

__global__ __launch_bounds__(512, 2) void fused_kernel(const u16* __restrict__ x, const u16* __restrict__ cr,
                                                       const u16* __restrict__ ci, const u16* __restrict__ dr,
                                                       const u16* __restrict__ di, const u16* __restrict__ wr,
                                                       const u16* __restrict__ wg_, const u16* __restrict__ wu_,
                                                       const u16* __restrict__ wd_, unsigned char* ws, u16* out) {
  extern __shared__ __attribute__((aligned(16))) unsigned char smem[];
  LAS unsigned char* L = (LAS unsigned char*)smem;
  const int tid = threadIdx.x, w8 = __builtin_amdgcn_readfirstlane(tid >> 6), w = w8 & 3, l = tid & 63, r = l & 31, h = l >> 5;
  const int b = blockIdx.x, xcd = b & 7, j = b >> 3;
  TSDECL STAMP(0);
  const unsigned epoch_v = __hip_atomic_load((GAS unsigned*)(ws + WS_FLAG + FL_EPOCH), RLX_AGENT);

  const int isB = xcd >> 2, sb = ((xcd >> 1) & 1) * 8 + (xcd & 1) * 4 + (j & 3), e8 = j >> 2;
  const u16* dm = (isB ? di : dr) + (size_t)e8 * 64 * D_;
  const auto rx = RSRC(x + (size_t)sb * 128 * D_, 128 * 1024);
  const auto rd = RSRC(dm, 64 * 1024);
  const int sz0 = (((l & 7) ^ ((l >> 4) & 7)) * 16), sz1 = (((l & 7) ^ ((4 + (l >> 4)) & 7)) * 16);
  const int v0 = (l >> 3) * 1024 + sz0, v1 = (l >> 3) * 1024 + sz1 + 7 * 1024;
  const int rot1 = (e8 + 2 * (j & 3)) & 7;
  auto issue = [&](int t_, int buf) {
    const int ko = ((t_ + rot1) & 7) * 128;
    LAS unsigned char* bx = L + LD_P1 + buf * P1_BUF + w * 4096;
    LAS unsigned char* bd = L + LD_P1 + buf * P1_BUF + 16384 + w * 2048;
    DMA(rd, bd, v0, 16 * w * 1024 + ko, 0, K1AUX);
    DMA(rd, bd, v1, 16 * w * 1024 + ko, 1024, K1AUX);
    DMA(rx, bx, v0, 32 * w * 1024 + ko, 0, K1AUX);
    DMA(rx, bx, v1, 32 * w * 1024 + ko, 1024, K1AUX);
    DMA(rx, bx, v0, 32 * w * 1024 + ko + 14 * 1024, 2048, K1AUX);
    DMA(rx, bx, v1, 32 * w * 1024 + ko + 14 * 1024, 3072, K1AUX);
  };
  const int srow = (isB * 8 + e8) * 8 + w8;
  u32x4 xr = __builtin_amdgcn_raw_buffer_load_b128(rx, l * 16, srow * 1024, 0);
  issue(w8 >> 2, w8 >> 2);
  if (tid < 4) ((LAS unsigned*)(L + SC_CTL))[tid] = 0u;
  STAMP(1);
  const int ks = xcd >> 1, mb = (xcd & 1) * 8 + (j >> 2), nb = j & 3;
  const u16* A = ((ks >> 1) ? ci : cr) + (size_t)mb * 128 * S_ + (ks & 1) * 1024;
  const auto ra = RSRC(A, 127 * 4096 + 2048);
  const auto rb = RSRC(ws + WS_AB + (size_t)(nb * 64 + ks * 16) * 16384, 16 * 16384);
  const int va0 = (32 * w + (l >> 3)) * 4096 + sz0, va1 = (32 * w + (l >> 3)) * 4096 + sz1;
  const int vb = w * 4096 + l * 16;
  const int rot2 = (4 * nb + 2 * (mb & 7)) & 15;
  auto stageA = [&](int t_, int buf) {
    const int t = (t_ + rot2) & 15;
    DMA(ra, L + LD_A + buf * 16384 + w * 4096, va0, t * 128, 0, AAUX);
    DMA(ra, L + LD_A + buf * 16384 + w * 4096, va1, t * 128 + 1 * (32768 - 1024), 1024, AAUX);
    DMA(ra, L + LD_A + buf * 16384 + w * 4096, va0, t * 128 + 2 * (32768 - 1024), 2048, AAUX);
    DMA(ra, L + LD_A + buf * 16384 + w * 4096, va1, t * 128 + 3 * (32768 - 1024), 3072, AAUX);
  };
  auto stageB = [&](int t_, int buf) {
    const int t = (t_ + rot2) & 15;
    DMA(rb, L + LD_B + buf * 16384 + w * 4096, vb, t * 16384, 0, BAUX);
    DMA(rb, L + LD_B + buf * 16384 + w * 4096, vb, t * 16384, 1024, BAUX);
    DMA(rb, L + LD_B + buf * 16384 + w * 4096, vb, t * 16384, 2048, BAUX);
    DMA(rb, L + LD_B + buf * 16384 + w * 4096, vb, t * 16384, 3072, BAUX);
  };
  const int i16 = l & 15, q4 = l >> 4;
  unsigned tag;
  u64 tag64;
  auto make_tag = [&]() {
    const unsigned e = __builtin_amdgcn_readfirstlane(epoch_v);
    tag = (e + 1u) * 0x9E3779B1u ^ 0x5BD1E995u;
    tag64 = ((u64)tag << 32) | (u64)(e * 0x85EBCA77u + 0x27D4EB2Fu);
  };
  auto wave_sum = [&](float v) {
    v += dpp<XOR1>(v); v += dpp<XOR2>(v); v += dpp<XOR7>(v); v += dpp<XOR8>(v);
    v = fold16(v, v); return fold32(v, v);
  };
  auto row_stats = [&]() {
    asm volatile("" : "+v"(xr));
    float xv[8];
#pragma unroll
    for (int i = 0; i < 4; ++i) { xv[2 * i] = bf_lo(xr[i]); xv[2 * i + 1] = bf_hi(xr[i]); }
    const float mean = wave_sum(((xv[0] + xv[1]) + (xv[2] + xv[3])) + ((xv[4] + xv[5]) + (xv[6] + xv[7]))) * (1.0f / D_);
    float q = 0.f;
#pragma unroll
    for (int i = 0; i < 8; ++i) { const float d = xv[i] - mean; q = fmaf(d, d, q); }
    const float rs = __builtin_amdgcn_rsqf(wave_sum(q) * (1.0f / D_) + 1e-5f);
    if (l < 2) __hip_atomic_store((GAS u64*)(ws + WS_FLAG + FL_STAT) + (sb * 128 + srow) * 2 + l, ((u64)tag << 32) | (u64)__float_as_uint(l ? rs : mean * rs), RLX_AGENT);
  };

  if (w8 >= 4) {
    issue(2, 2);
    f32x4 sdv = {};
    const bf16x8 ones = {0x3f80, 0x3f80, 0x3f80, 0x3f80, 0x3f80, 0x3f80, 0x3f80, 0x3f80};
    const int sdd = 16384 + (w * 16 + i16) * 128;
    const int ps0 = (q4 ^ (i16 >> 1)) * 16, ps1 = ((4 + q4) ^ (i16 >> 1)) * 16;
#pragma unroll
    for (int t = 0; t < 8; ++t) {
      if (t >= 1) waitv_n(p1_younger(t));
      WAITL();
      BAR();
      const LAS unsigned char* bb = L + LD_P1 + (t & 3) * P1_BUF;
      const bf16x8 d0 = *(const LAS bf16x8*)(bb + sdd + ps0), d1 = *(const LAS bf16x8*)(bb + sdd + ps1);
      SB();
      if (t < 5) issue(t + 3, (t + 3) & 3);
      if (t >= AE && t < AE + 3) stageA(t - AE, t - AE);
      if (t == AE + 3)
        __builtin_amdgcn_raw_ptr_buffer_load_lds(ra, (LAS void*)(L + SC_PF + w * 256), 4, l < 32 ? (32 * w + l) * 4096 : 0x7ffffff0, ((3 + rot2) & 15) * 128, 0, AAUX);
      if (t == 0) { make_tag(); row_stats(); }
      SB();
      sdv = MFMA16(d0, ones, sdv);
      sdv = MFMA16(d1, ones, sdv);
      SB();
    }
    if (i16 == 0) *(LAS f32x4*)(L + SC_SD + (w * 16 + q4 * 4) * 4) = sdv;
    WAITL();
    BAR();
    STAMP(2);
    stageA(3, 3);
    if (w8 == 4) {
      GAS u64* fl = (GAS u64*)(ws + WS_FLAG + FL_AB) + ((ks * 16 + mb) * 4 + nb) * 64 + ((WGFLAG & 1) ? (l & 15) : l);
      if (ABNAP) __builtin_amdgcn_s_sleep(ABNAP);
      for (unsigned spins = 0;; ++spins) {
        if (__all(__hip_atomic_load(fl, RLX_AGENT) == tag64) || spins > SPIN_CAP) { TMO(6); break; }
        __builtin_amdgcn_s_sleep(1);
      }
    }
    WAITV(0);
    BAR();
    STAMP(3);
    stageB(1, 1);
    BAR();
    stageB(2, 2); stageB(3, 3);
    WAITV(8); BAR(); stageA(4, 0); stageB(4, 0);
    WAITV(12); BAR(); stageA(5, 1); stageB(5, 1);
#pragma unroll
    for (int t = 2; t < 12; ++t) {
      WAITV(16);
      BAR();
      stageA(t + 4, t & 3); stageB(t + 4, t & 3);
    }
    {
      const bool rowrole = (j & 1) == 0;
      const auto rxr = RSRC(x + (size_t)((mb * 8 + ks * 2 + (nb >> 1)) * 16) * D_, rowrole ? 16 * 1024 : 0);
      const auto rwr = RSRC(wr, rowrole ? 1024 : 0);
#pragma unroll
      for (int i = 0; i < 4; ++i) DMA(rxr, L + LD_XR + (4 * w + i) * 1024, (l ^ (4 * w + i)) * 16, (4 * w + i) * 1024, 0, 0);
      DMA(rwr, L + SC_PF, l * 16, 0, 0, 0);
    }
    WAITV(21); BAR(); WAITV(13); BAR(); WAITV(5); BAR();
    WAITV(0);
    STAMP(5);
    if (j & 1) {
      BAR();
      const int o2 = j >> 1;
      if (o2 < 7) {
        const int fp = xcd * 7 + o2;
        const auto rg = RSRC(wg_, D_ * F_ * 2);
        const auto ru = RSRC(wu_, D_ * F_ * 2);
        const auto rw = RSRC(wd_ + (size_t)fp * 32 * D_, 32 * D_ * 2);
        const int vg = (w * 16 + (l >> 2)) * (F_ * 2) + fp * 64;
        const int cg = (l & 3) ^ (2 * (l >> 5));
#pragma unroll
        for (int i = 0; i < 8; ++i) {
          DMA(rg, L + (i * 4 + w) * 1024, vg + cg * 16, i * 64 * F_ * 2, 0, WAUX);
          DMA(ru, L + 32768 + (i * 4 + w) * 1024, vg + (cg ^ 1) * 16, i * 64 * F_ * 2, 0, WAUX);
        }
#pragma unroll
        for (int i = 0; i < 8; ++i) DMA(rw, L + 65536 + (w * 8 + i) * 1024, (l ^ (2 * (i & 3) + 8 * (w & 1))) * 16, (w * 8 + i) * 1024, 0, WAUX);
      } else if (HELP) {
#pragma unroll
        for (int i = 0; i < 2; ++i) {
          const int slot = ((o2 - 7) * 4 + w) * 2 + i;
          const bool gu = slot < 32;
          const u16* wp = gu ? ((slot & 1) ? wu_ : wg_) : wd_ + (size_t)xcd * 224 * D_;
          const auto rq = RSRC(wp, gu ? D_ * F_ * 2 : slot < 60 ? 224 * D_ * 2 : 0);
          const int vo = gu ? (256 + (slot >> 1) * 16 + (l >> 2)) * (F_ * 2) + ((xcd * 448) & ~127) + (l & 3) * 128 : ((slot - 32) * 64 + l) * 128;
          __builtin_amdgcn_raw_ptr_buffer_load_lds(rq, (LAS void*)(L + w * 256), 4, vo, 0, 0, WAUX);
        }
      }
    }
  } else {
    {
      f32x16 acc0 = {}, acc1 = {};
      bf16x8 fx[2][4], f0[2][4], f1[2][4];
      int pos[4];
#pragma unroll
      for (int kk = 0; kk < 4; ++kk) pos[kk] = r * 128 + (((2 * kk + h) ^ ((r >> 1) & 7)) * 16);
      auto ldf = [&](int t, int kk) {
        const LAS unsigned char* bb = L + LD_P1 + (t & 3) * P1_BUF;
        fx[t & 1][kk] = *(const LAS bf16x8*)(bb + w * 4096 + pos[kk]);
        f0[t & 1][kk] = *(const LAS bf16x8*)(bb + 16384 + pos[kk]);
        f1[t & 1][kk] = *(const LAS bf16x8*)(bb + 16384 + 4096 + pos[kk]);
      };
      make_tag();
      row_stats();
      WAITV(1);
      BAR();
      STAMP(2);
#pragma unroll
      for (int kk = 0; kk < 4; ++kk) ldf(0, kk);
      GAS u64* stp = (GAS u64*)(ws + WS_FLAG + FL_STAT) + (sb * 128 + w * 32) * 2 + l;
      u64 sg = 0;
#pragma unroll
      for (int t = 0; t < 8; ++t) {
        const int p = t & 1;
        if (t < 7) { WAITL(); BAR(); }
        if (t == STAT_T) { sg = __hip_atomic_load(stp, RLX_AGENT); SB(); }
#pragma unroll
        for (int kk = 0; kk < 4; ++kk) {
          if (t < 7) ldf(t + 1, kk);
          SB();
          acc0 = MFMA32(fx[p][kk], f0[p][kk], acc0);
          acc1 = MFMA32(fx[p][kk], f1[p][kk], acc1);
          SB();
        }
      }
      LAS float* st = (LAS float*)(L + SC_ST + w * 512);
      for (unsigned spins = 0; !__all((unsigned)(sg >> 32) == tag) && spins <= SPIN_CAP; ++spins) sg = __hip_atomic_load(stp, RLX_AGENT);
      st[l] = __uint_as_float((unsigned)sg ^ (isB ? 0x80000000u : 0u));
      WAITL();
      BAR();
      STAMP(16);
      const LAS float* sdl = (const LAS float*)(L + SC_SD);
      const float cs0 = sdl[r], cs1 = sdl[32 + r];
      const LAS f32x4* st4 = (const LAS f32x4*)st;
      unsigned p0[8], p1[8];
#pragma unroll
      for (int g = 0; g < 4; ++g) {
        const f32x4 sa = st4[4 * g + 2 * h], sbv = st4[4 * g + 2 * h + 1];
        p0[2 * g] = pk_bf16(acc0[4 * g] * sa[1] - sa[0] * cs0, acc0[4 * g + 1] * sa[3] - sa[2] * cs0);
        p0[2 * g + 1] = pk_bf16(acc0[4 * g + 2] * sbv[1] - sbv[0] * cs0, acc0[4 * g + 3] * sbv[3] - sbv[2] * cs0);
        p1[2 * g] = pk_bf16(acc1[4 * g] * sa[1] - sa[0] * cs1, acc1[4 * g + 1] * sa[3] - sa[2] * cs1);
        p1[2 * g + 1] = pk_bf16(acc1[4 * g + 2] * sbv[1] - sbv[0] * cs1, acc1[4 * g + 3] * sbv[3] - sbv[2] * cs1);
      }
      const int kk0 = isB * 2048 + sb * 128 + w * 32;
      const int kb = kk0 >> 6, kst = (kk0 >> 4) & 3;
      const int nbp = e8 >> 1, nt0 = (e8 & 1) * 2;
      const auto rab = RSRC(ws + WS_AB + (size_t)(nbp * 64 + kb) * 16384, 16384);
#pragma unroll
      for (int s2 = 0; s2 < 2; ++s2) {
        {
          auto t0 = __builtin_amdgcn_permlane32_swap(p0[4 * s2], p0[4 * s2 + 2], false, false);
          auto t1 = __builtin_amdgcn_permlane32_swap(p0[4 * s2 + 1], p0[4 * s2 + 3], false, false);
          u32x4 o = {t0[0], t1[0], t0[1], t1[1]};
          __builtin_amdgcn_raw_buffer_store_b128(o, rab, l * 16, ((kst + s2) * 4 + nt0) * 1024, AUX_SC1);
        }
        {
          auto t0 = __builtin_amdgcn_permlane32_swap(p1[4 * s2], p1[4 * s2 + 2], false, false);
          auto t1 = __builtin_amdgcn_permlane32_swap(p1[4 * s2 + 1], p1[4 * s2 + 3], false, false);
          u32x4 o = {t0[0], t1[0], t0[1], t1[1]};
          __builtin_amdgcn_raw_buffer_store_b128(o, rab, l * 16, ((kst + s2) * 4 + nt0 + 1) * 1024, AUX_SC1);
        }
      }
      STAMP(17);
      WAITV(0);
      unsigned arr1 = 0;
      if (l == 0) arr1 = __hip_atomic_fetch_add((LAS unsigned*)(L + SC_CTL), 1u, __ATOMIC_RELAXED, __HIP_MEMORY_SCOPE_WORKGROUP);
      if (__builtin_amdgcn_readfirstlane(arr1) == 3u && l < 16)
        __hip_atomic_store((GAS u64*)(ws + WS_FLAG + FL_AB) + (((isB * 2 + (sb >> 3)) * 16 + l) * 4 + nbp) * 64 + (sb & 7) * 2 + (e8 & 1), tag64, RLX_AGENT);
      STAMP(18);
    }
    const int wm = w >> 1, wn = w & 1;
    f32x16 acc[2][2] = {};
    int aoff[4];
    const int sw = (r >> 1) & 7;
#pragma unroll
    for (int kk = 0; kk < 4; ++kk) aoff[kk] = LD_A + (wm * 64 + r) * 128 + (((2 * kk + h) ^ sw) * 16);
    const int boff = LD_B + wn * 2048 + l * 16;
    bf16x8 fa[2][2], fb[2][2];
    auto ldf = [&](int t, int kk, int p) {
      const int bo = (t & 3) * 16384;
      fb[p][0] = *(const LAS bf16x8*)(L + bo + boff + kk * 4096);
      fa[p][0] = *(const LAS bf16x8*)(L + bo + aoff[kk]);
      fa[p][1] = *(const LAS bf16x8*)(L + bo + aoff[kk] + 4096);
      fb[p][1] = *(const LAS bf16x8*)(L + bo + boff + kk * 4096 + 1024);
    };
    auto mma = [&](int p) {
      acc[0][0] = MFMA32(fb[p][0], fa[p][0], acc[0][0]);
      acc[1][0] = MFMA32(fb[p][0], fa[p][1], acc[1][0]);
      acc[0][1] = MFMA32(fb[p][1], fa[p][0], acc[0][1]);
      acc[1][1] = MFMA32(fb[p][1], fa[p][1], acc[1][1]);
      SB();
    };
    BAR();
    STAMP(3);
    stageB(0, 0);
    WAITV(0);
    BAR();
    STAMP(4);
    ldf(0, 0, 0);
#pragma unroll
    for (int t = 0; t < 16; ++t) {
      ldf(t, 1, 1); SB(); mma(0);
      ldf(t, 2, 0); SB(); mma(1);
      ldf(t, 3, 1); SB(); mma(0);
      if (t < 15) { WAITL(); BAR(); ldf(t + 1, 0, 0); SB(); }
      mma(1);
    }
    STAMP(5);
    const auto rp = RSRC(ws + WS_PART + (size_t)((ks * 16 + mb) * 4 + nb) * 32768, 32768);
#pragma unroll
    for (int nt = 0; nt < 2; ++nt)
#pragma unroll
      for (int mt = 0; mt < 2; ++mt)
#pragma unroll
        for (int s2 = 0; s2 < 2; ++s2) {
          const f32x16& a = acc[mt][nt];
          const unsigned p0 = pk_bf16(a[8 * s2], a[8 * s2 + 1]), p1 = pk_bf16(a[8 * s2 + 2], a[8 * s2 + 3]);
          const unsigned p2 = pk_bf16(a[8 * s2 + 4], a[8 * s2 + 5]), p3 = pk_bf16(a[8 * s2 + 6], a[8 * s2 + 7]);
          auto t0 = __builtin_amdgcn_permlane32_swap(p0, p2, false, false);
          auto t1 = __builtin_amdgcn_permlane32_swap(p1, p3, false, false);
          const u32x4 o = {t0[0], t1[0], t0[1], t1[1]};
          __builtin_amdgcn_raw_buffer_store_b128(o, rp, (((w * 2 + mt) * 2 + nt) * 2 + s2) * 1024 + l * 16, 0, AUX_SC1);
          SB();
        }
    STAMP(6);
    if (j & 1) BAR();
    WAITV(0);
    unsigned arr2 = 0;
    if (l == 0) arr2 = __hip_atomic_fetch_add((LAS unsigned*)(L + SC_CTL) + 1 + wm, 1u, __ATOMIC_RELAXED, __HIP_MEMORY_SCOPE_WORKGROUP);
    if (__builtin_amdgcn_readfirstlane(arr2) == 1u && l < 4)
      __hip_atomic_store((GAS u64*)(ws + WS_FLAG + FL_ARR) + (mb * 8 + wm * 4 + l) * 32 + ks * 4 + nb, tag64, RLX_AGENT);
    STAMP(7);
  }
  GAS u64* cand = (GAS u64*)(ws + WS_FLAG + FL_CAND);
  const auto ro = RSRC(out, S_ * D_ * 2);
  const u64 thi = (u64)tag << 32;

  if ((j & 1) == 0) {
    const int q2 = ks * 2 + (nb >> 1), ci_ = mb * 8 + q2;
    const int r16 = l & 15, c = tid >> 4;
    const int row = ci_ * 16 + r16;
    BAR();
    u32x4 xv[2], wv[2];
#pragma unroll
    for (int u = 0; u < 2; ++u) {
      xv[u] = *(const LAS u32x4*)(L + LD_XR + r16 * 1024 + (((c + 32 * u) ^ r16) * 16));
      wv[u] = *(const LAS u32x4*)(L + SC_PF + (c + 32 * u) * 16);
    }
    if (w8 == 5) {
      GAS u64* arr = (GAS u64*)(ws + WS_FLAG + FL_ARR) + ci_ * 32 + ((WGFLAG & 2) ? (l & 15) : r);
      if (ARNAP) __builtin_amdgcn_s_sleep(ARNAP);
      for (unsigned spins = 0;; ++spins) {
        if (__all(__hip_atomic_load(arr, RLX_AGENT) == tag64) || spins > SPIN_CAP) { TMO(0); break; }
        __builtin_amdgcn_s_sleep(1);
      }
    }
    const auto rp = RSRC(ws + WS_PART, 256 * 32768);
    const int poff = (mb * 4 + (c >> 4)) * 32768 +
                     (((((q2 >> 2) * 2 + ((c >> 3) & 1)) * 2 + ((q2 >> 1) & 1)) * 2 + ((c >> 2) & 1)) * 2 + ((c >> 1) & 1)) * 1024 +
                     ((c & 1) * 32 + (q2 & 1) * 16 + r16) * 16;
    SB();
    BAR();
    STAMP(8);
    u32x4 pv[2][4];
#pragma unroll
    for (int u = 0; u < 2; ++u)
#pragma unroll
      for (int k2 = 0; k2 < 4; ++k2) pv[u][k2] = __builtin_amdgcn_raw_buffer_load_b128(rp, poff, (k2 * 64 + u * 2) * 32768, AUX_SC1);
    float sw = 0.f, wf[16], hv[16];
#pragma unroll
    for (int u = 0; u < 2; ++u)
#pragma unroll
      for (int i = 0; i < 4; ++i) {
        wf[8 * u + 2 * i] = bf_lo(wv[u][i]); wf[8 * u + 2 * i + 1] = bf_hi(wv[u][i]);
        sw += wf[8 * u + 2 * i] + wf[8 * u + 2 * i + 1];
        hv[8 * u + 2 * i] = bf_lo(xv[u][i]) + ((bf_lo(pv[u][0][i]) + bf_lo(pv[u][1][i])) + (bf_lo(pv[u][2][i]) + bf_lo(pv[u][3][i])));
        hv[8 * u + 2 * i + 1] = bf_hi(xv[u][i]) + ((bf_hi(pv[u][0][i]) + bf_hi(pv[u][1][i])) + (bf_hi(pv[u][2][i]) + bf_hi(pv[u][3][i])));
      }
    float mu = 0.f, m2 = 0.f, hw = 0.f;
#pragma unroll
    for (int i = 0; i < 4; ++i) mu += (hv[4 * i] + hv[4 * i + 1]) + (hv[4 * i + 2] + hv[4 * i + 3]);
    mu *= 0.0625f;
    STAMP(9);
#pragma unroll
    for (int i = 0; i < 16; ++i) { const float d = hv[i] - mu; m2 = fmaf(d, d, m2); hw = fmaf(hv[i], wf[i], hw); }
    float hs = fold16(hw, sw);
    {
      auto t = __builtin_amdgcn_permlane16_swap(__float_as_uint(mu), __float_as_uint(mu), false, false);
      const float a = __uint_as_float(t[0]), bb = __uint_as_float(t[1]), d = a - bb;
      m2 = fold16(m2, m2) + d * d * 8.0f; mu = (a + bb) * 0.5f;
    }
    hs = fold32(hs, hs);
    {
      auto t = __builtin_amdgcn_permlane32_swap(__float_as_uint(mu), __float_as_uint(mu), false, false);
      const float a = __uint_as_float(t[0]), bb = __uint_as_float(t[1]), d = a - bb;
      m2 = fold32(m2, m2) + d * d * 16.0f; mu = (a + bb) * 0.5f;
    }
    LAS float* red = (LAS float*)(L + SC_RED);
    if (l < 16) *(LAS f32x2*)(red + (w8 * 16 + l) * 4) = (f32x2){mu, m2};
    if (l < 32) red[(w8 * 16 + r16) * 4 + 2 + (l >> 4)] = hs;
    WAITL();
    BAR();
    STAMP(10);
    float mean, M2, HW, SW;
    {
      const f32x4 ta = *(const LAS f32x4*)(red + l * 4), tb = *(const LAS f32x4*)(red + (64 + l) * 4);
      float ms = ta[0] + tb[0];
      float hs2 = fold16(ta[2] + tb[2], ta[3] + tb[3]);
      ms = fold16(ms, ms); ms = fold32(ms, ms);
      mean = ms * 0.125f;
      const float da = ta[0] - mean, db = tb[0] - mean;
      float q = (ta[1] + tb[1]) + 64.0f * (da * da + db * db);
      hs2 = fold32(hs2, hs2);
      q = fold16(q, q); M2 = fold32(q, q);
      auto t = __builtin_amdgcn_permlane16_swap(__float_as_uint(hs2), __float_as_uint(hs2), false, false);
      HW = __uint_as_float(t[0]); SW = __uint_as_float(t[1]);
    }
    const float rstd = __builtin_amdgcn_rsqf(M2 * (1.0f / D_) + 1e-5f);
    const float rr = (HW - mean * SW) * rstd;
    unsigned key = (ord_f32(rr) & ~15u) | (unsigned)(15 - r16);
    key = max(key, dppu<XOR1>(key)); key = max(key, dppu<XOR2>(key));
    key = max(key, dppu<XOR7>(key)); key = max(key, dppu<XOR8>(key));
    if (tid < 16) __hip_atomic_store(cand + tid * 128 + ci_, thi | (u64)key, RLX_AGENT);
    STAMP(11);
    const bool best = (15 - (int)(key & 15u)) == r16;
    float hn[16], ob[16];
#pragma unroll
    for (int i = 0; i < 16; ++i) { hn[i] = (hv[i] - mean) * rstd; ob[i] = hv[i] + hn[i]; }
    LAS unsigned* rowh = (LAS unsigned*)(L + SC_GU);
    LAS float* rowb = (LAS float*)(L + SC_X);
    if (best) {
#pragma unroll
      for (int u = 0; u < 2; ++u) {
        *(LAS u32x4*)(rowh + (c + 32 * u) * 4) = (u32x4){pk_bf16(hn[8 * u], hn[8 * u + 1]), pk_bf16(hn[8 * u + 2], hn[8 * u + 3]),
                                                         pk_bf16(hn[8 * u + 4], hn[8 * u + 5]), pk_bf16(hn[8 * u + 6], hn[8 * u + 7])};
        *(LAS f32x4*)(rowb + (c + 32 * u) * 8) = (f32x4){ob[8 * u], ob[8 * u + 1], ob[8 * u + 2], ob[8 * u + 3]};
        *(LAS f32x4*)(rowb + (c + 32 * u) * 8 + 4) = (f32x4){ob[8 * u + 4], ob[8 * u + 5], ob[8 * u + 6], ob[8 * u + 7]};
      }
    }
    WAITL();
    BAR();
    if (w8 < 4) __hip_atomic_store((GAS u64*)(ws + WS_FLAG + FL_HN + ci_ * 2048) + tid, thi | (u64)rowh[tid], RLX_AGENT);
    if (w8 == 7) {
      const f32x4 a = *(const LAS f32x4*)(rowb + l * 8), bq = *(const LAS f32x4*)(rowb + l * 8 + 4);
      const u32x4 o = {pk_bf16(a[0], a[1]), pk_bf16(a[2], a[3]), pk_bf16(bq[0], bq[1]), pk_bf16(bq[2], bq[3])};
      __builtin_amdgcn_raw_buffer_store_b128(o, ro, ((ci_ * 16 + 15 - (int)(key & 15u)) * D_ + l * 8) * 2, 0, AUX_SC1);
    }
    __hip_atomic_store((GAS u64*)(ws + WS_BASE + ci_ * 4096) + tid, thi | (u64)__float_as_uint(rowb[tid]), RLX_AGENT);
    if (w8 == 7) {
      WAITV(0);
      if (l == 0) __hip_atomic_store((GAS u64*)(ws + WS_FLAG + FL_DONE) + ci_ * 16, tag64, RLX_AGENT);
    }
    SB();
    if (!best) {
#pragma unroll
      for (int u = 0; u < 2; ++u) {
        const u32x4 o = {pk_bf16(ob[8 * u], ob[8 * u + 1]), pk_bf16(ob[8 * u + 2], ob[8 * u + 3]),
                         pk_bf16(ob[8 * u + 4], ob[8 * u + 5]), pk_bf16(ob[8 * u + 6], ob[8 * u + 7])};
        __builtin_amdgcn_raw_buffer_store_b128(o, ro, (row * D_ + (c + 32 * u) * 8) * 2, 0, OUTAUX);
      }
    }
    STAMP(12);
    TSDUMP(1, w8);
    return;
  }
  const int o2 = j >> 1;
  if (o2 < 7) {
    const int fp = xcd * 7 + o2;
    STAMP(8);
    bf16x8 gf[16], df[4];
    WAITV(0);
    BAR();
    {
      const int q = (l & 15) >> 2, p = l & 3;
      if (w8 < 4) {
        const LAS unsigned char* pg = L + (p >> 1) * 32768 + (8 * q4 + q) * 64 + ((w ^ (2 * (q4 & 1)) ^ (p >> 1)) * 16) + (p & 1) * 8;
#pragma unroll
        for (int k2 = 0; k2 < 16; ++k2) gf[k2] = tr_frag(pg + k2 * 2048, pg + k2 * 2048 + 256);
      } else {
#pragma unroll
        for (int k2 = 0; k2 < 16; ++k2) gf[k2] = (bf16x8){};
      }
      const LAS unsigned char* pd = L + 65536 + (8 * q4 + q) * 1024 + (p & 1) * 8;
#pragma unroll
      for (int t = 0; t < 4; ++t) {
        const int ch = ((w8 * 8 + t * 2 + (p >> 1)) ^ (2 * q + 8 * (q4 & 1))) * 16;
        df[t] = tr_frag(pd + ch, pd + ch + 4096);
      }
#pragma unroll
      for (int k2 = 0; k2 < 16; ++k2) asm volatile("" : "+v"(gf[k2]));
#pragma unroll
      for (int t = 0; t < 4; ++t) asm volatile("" : "+v"(df[t]));
    }
    STAMP(9);
    if (w8 == 4) {
      GAS u64* cp = cand + xcd * 128 + l;
      int lead = 0;
      for (unsigned spins = 0;; ++spins) {
        const u64 c0 = __hip_atomic_load(cp, RLX_AGENT), c1 = __hip_atomic_load(cp + 64, RLX_AGENT);
        unsigned key; int nin;
        best_candidate(c0, c1, tag, key, lead, nin);
        if (nin >= LIGHT_TH || spins > SPIN_CAP) break;
        __builtin_amdgcn_s_sleep(LIGHT_SL);
      }
      for (unsigned spins = 0;; ++spins) {
        const u64 c0 = __hip_atomic_load(cp, RLX_AGENT), c1 = __hip_atomic_load(cp + 64, RLX_AGENT);
        GAS u64* hp = (GAS u64*)(ws + WS_FLAG + FL_HN + lead * 2048) + l * 4;
        const u64 g0 = __hip_atomic_load(hp, RLX_AGENT), g1 = __hip_atomic_load(hp + 1, RLX_AGENT);
        const u64 g2 = __hip_atomic_load(hp + 2, RLX_AGENT), g3 = __hip_atomic_load(hp + 3, RLX_AGENT);
        unsigned key; int wc, nin;
        best_candidate(c0, c1, tag, key, wc, nin);
        *(LAS u32x4*)(L + SC_X + l * 16) = (u32x4){(unsigned)g0, (unsigned)g1, (unsigned)g2, (unsigned)g3};
        const bool ok = ((unsigned)(g0 >> 32) == tag) & ((unsigned)(g1 >> 32) == tag) & ((unsigned)(g2 >> 32) == tag) & ((unsigned)(g3 >> 32) == tag);
        if ((nin == 128 && wc == lead && __all(ok)) || spins > SPIN_CAP) { TMO(1); break; }
        lead = wc;
        if (nin < 128) __builtin_amdgcn_s_sleep(1);
      }
    }
    WAITL();
    BAR();
    STAMP(11);
    if (w8 < 4) {
      f32x4 gu = {};
#pragma unroll
      for (int k2 = 0; k2 < 16; ++k2) gu = MFMA16(*(const LAS bf16x8*)(L + SC_X + k2 * 64 + q4 * 16), gf[k2], gu);
      const float g = gu[0], u = dpp<XOR8>(g);
      const float act = g * __builtin_amdgcn_rcpf(1.0f + __expf(-g)) * u;
      const unsigned pr = pk_bf16(act, dpp<XOR1>(act));
      if ((l & 0x39) == 0) ((LAS unsigned*)(L + SC_ACT))[w * 4 + (l >> 1)] = pr;
    }
    WAITL();
    BAR();
    {
      const bf16x8 af = *(const LAS bf16x8*)(L + SC_ACT + q4 * 16);
      const f32x4 z = {};
      const f32x4 y0 = MFMA16(af, df[0], z), y1 = MFMA16(af, df[1], z), y2 = MFMA16(af, df[2], z), y3 = MFMA16(af, df[3], z);
      const unsigned m0 = 0u - (unsigned)(q4 & 1), m1 = 0u - (unsigned)(q4 >> 1);
      const float y = bsel(m1, bsel(m0, y3[0], y2[0]), bsel(m0, y1[0], y0[0]));
      const float yn = dpp<XOR1>(y);
      if ((l & 1) == 0) {
        const int cp = tid >> 1;
        __hip_atomic_store((GAS u64*)(ws + WS_FLAG + FL_Y2 + ((cp >> 3) * 56 + fp) * 64) + (cp & 7), thi | (u64)pk_bf16(y, yn), RLX_AGENT);
      }
    }
    STAMP(12);
  } else if (o2 < 11 && w8 == 4) {
    const int cb = xcd * 4 + (o2 - 7);
    unsigned whi;
    u64 b0, b1;
    float y0, y1;
    int lead = 0;
    GAS u64* cp = cand + (8 + xcd) * 128 + l;
    for (int i = 0; i < FINAL_NAP; ++i) __builtin_amdgcn_s_sleep(64);
    for (unsigned spins = 0;; ++spins) {
      const u64 c0 = __hip_atomic_load(cp, RLX_AGENT), c1 = __hip_atomic_load(cp + 64, RLX_AGENT);
      int nin;
      best_candidate(c0, c1, tag, whi, lead, nin);
      if (nin == 128 || spins > SPIN_CAP) { TMO(5); break; }
      __builtin_amdgcn_s_sleep(4);
    }
    STAMP(10);
    const int wrow = lead * 16 + 15 - (int)(whi & 15u);
    const float sig = 1.0f / (1.0f + __expf(-unord_f32(whi)));
    for (unsigned spins = 0;; ++spins) {
      u64 g[7];
#pragma unroll
      for (int i = 0; i < 7; ++i) g[i] = __hip_atomic_load((GAS u64*)(ws + WS_FLAG + FL_Y2 + cb * 56 * 64) + l + i * 64, RLX_AGENT);
      const u64 dn = __hip_atomic_load((GAS u64*)(ws + WS_FLAG + FL_DONE) + lead * 16, RLX_AGENT);
      GAS u64* bp = (GAS u64*)(ws + WS_BASE + lead * 4096) + cb * 16 + (l & 7) * 2;
      b0 = __hip_atomic_load(bp, RLX_AGENT); b1 = __hip_atomic_load(bp + 1, RLX_AGENT);
      bool ok = (dn == tag64) & ((unsigned)(b0 >> 32) == tag) & ((unsigned)(b1 >> 32) == tag);
      y0 = 0.f; y1 = 0.f;
#pragma unroll
      for (int i = 0; i < 7; ++i) { ok &= (unsigned)(g[i] >> 32) == tag; y0 += bf_lo((unsigned)g[i]); y1 += bf_hi((unsigned)g[i]); }
      if (__all(ok) || spins > SPIN_CAP) { TMO(4); break; }
    }
    STAMP(13);
    y0 += dpp<XOR8>(y0); y1 += dpp<XOR8>(y1);
    float yy = fold16(y0, y1);
    yy = fold32(yy, yy);
    auto t = __builtin_amdgcn_permlane16_swap(__float_as_uint(yy), __float_as_uint(yy), false, false);
    const float f0 = __uint_as_float(t[0]), f1 = __uint_as_float(t[1]);
    if (l < 8)
      __builtin_amdgcn_raw_buffer_store_b32(pk_bf16(__uint_as_float((unsigned)b0) + f0 * sig, __uint_as_float((unsigned)b1) + f1 * sig),
                                            ro, (wrow * D_ + cb * 16 + l * 2) * 2, 0, AUX_SC1);
    if (cb == 0 && l == 0) __hip_atomic_store((GAS unsigned*)(ws + WS_FLAG + FL_EPOCH), epoch_v + 1u, RLX_AGENT);
    STAMP(14);
  }
  STAMP(15); TSDUMP(1, w8);
}

extern "C" void kernel_launch(void* const* d_in, const int* in_sizes, int n_in, void* d_out, int out_size, void* d_ws,
                              size_t ws_size, hipStream_t stream) {
  const u16* x = (const u16*)d_in[0];
  const u16* cr = (const u16*)d_in[1];
  const u16* ci = (const u16*)d_in[2];
  const u16* dr = (const u16*)d_in[3];
  const u16* di = (const u16*)d_in[4];
  const u16* wr = (const u16*)d_in[6];
  const u16* wg = (const u16*)d_in[7];
  const u16* wu = (const u16*)d_in[8];
  const u16* wd = (const u16*)d_in[9];
  (void)hipFuncSetAttribute((const void*)fused_kernel, hipFuncAttributeMaxDynamicSharedMemorySize, LDS_BYTES);
  fused_kernel<<<256, 512, LDS_BYTES, stream>>>(x, cr, ci, dr, di, wr, wg, wu, wd, (unsigned char*)d_ws, (u16*)d_out);
}
```

```cpp
#include <hip/hip_runtime.h>
#include <stdint.h>

typedef unsigned short u16;
typedef short bf16x8 __attribute__((ext_vector_type(8)));
typedef __bf16 bf16x2_t __attribute__((ext_vector_type(2)));
typedef float f32x16 __attribute__((ext_vector_type(16)));
typedef float f32x4 __attribute__((ext_vector_type(4)));
typedef float f32x2 __attribute__((ext_vector_type(2)));
typedef unsigned u32x4 __attribute__((ext_vector_type(4)));
typedef unsigned long long u64;
#define DI __device__ __forceinline__
#define LAS __attribute__((address_space(3)))
#define GAS __attribute__((address_space(1)))
#define MFMA32(a, b, c) __builtin_amdgcn_mfma_f32_32x32x16_bf16((a), (b), (c), 0, 0, 0)
#define MFMA16(a, b, c) __builtin_amdgcn_mfma_f32_16x16x32_bf16((a), (b), (c), 0, 0, 0)
#define DMA(rs, ldsp, voff, soff, imm, aux) __builtin_amdgcn_raw_ptr_buffer_load_lds(rs, (LAS void*)(ldsp), 16, voff, soff, imm, aux)
#define WAITV(n) asm volatile("s_waitcnt vmcnt(" #n ")" ::: "memory")
#define WAITL() asm volatile("s_waitcnt lgkmcnt(0)" ::: "memory")
#define BAR() do { asm volatile("" ::: "memory"); __builtin_amdgcn_s_barrier(); asm volatile("" ::: "memory"); } while (0)
#define SB() __builtin_amdgcn_sched_barrier(0)
#define RLX_AGENT __ATOMIC_RELAXED, __HIP_MEMORY_SCOPE_AGENT
#define RSRC(p, bytes) __builtin_amdgcn_make_buffer_rsrc((void*)(p), (short)0, (int)(bytes), 0x00020000)
constexpr int AUX_SC1 = 16;

constexpr int S_ = 2048, D_ = 512, F_ = 1792;
constexpr int A0W = 3, T4U = 6;
constexpr int TL = 6;

DI float bf_lo(unsigned w) { return __uint_as_float(w << 16); }
DI float bf_hi(unsigned w) { return __uint_as_float(w & 0xffff0000u); }
DI unsigned pk_bf16(float lo, float hi) {
  f32x2 v = {lo, hi};
  bf16x2_t b = __builtin_convertvector(v, bf16x2_t);
  return __builtin_bit_cast(unsigned, b);
}
template <int CTRL> DI float dpp(float x) {
  return __builtin_bit_cast(float, __builtin_amdgcn_mov_dpp(__builtin_bit_cast(int, x), CTRL, 0xf, 0xf, true));
}
template <int CTRL> DI unsigned dppu(unsigned x) { return (unsigned)__builtin_amdgcn_mov_dpp((int)x, CTRL, 0xf, 0xf, true); }
constexpr int XOR1 = 0xB1, XOR2 = 0x4E, XOR7 = 0x141, XOR8 = 0x128;
DI float fold32(float a, float b) {
  auto t = __builtin_amdgcn_permlane32_swap(__float_as_uint(a), __float_as_uint(b), false, false);
  return __uint_as_float(t[0]) + __uint_as_float(t[1]);
}
DI float fold16(float a, float b) {
  auto t = __builtin_amdgcn_permlane16_swap(__float_as_uint(a), __float_as_uint(b), false, false);
  return __uint_as_float(t[0]) + __uint_as_float(t[1]);
}
DI float bsel(unsigned m, float a, float b) { return __uint_as_float((m & __float_as_uint(a)) | (~m & __float_as_uint(b))); }
DI unsigned ord_f32(float v) { const unsigned u = __float_as_uint(v); return u ^ ((unsigned)((int)u >> 31) | 0x80000000u); }
DI float unord_f32(unsigned o) { return __uint_as_float(o ^ ((o >> 31) ? 0x80000000u : 0xffffffffu)); }

DI void best_candidate(u64 c0, u64 c1, unsigned tag, unsigned& key_out, int& slot, int& nin) {
  const bool v0 = (unsigned)(c0 >> 32) == tag, v1 = (unsigned)(c1 >> 32) == tag;
  const unsigned k0 = v0 ? (unsigned)c0 : 0u, k1 = v1 ? (unsigned)c1 : 0u;
  unsigned m = max(k0, k1);
  m = max(m, dppu<XOR1>(m)); m = max(m, dppu<XOR2>(m)); m = max(m, dppu<XOR7>(m)); m = max(m, dppu<XOR8>(m));
  { auto t = __builtin_amdgcn_permlane16_swap(m, m, false, false); m = max(t[0], t[1]); }
  { auto t = __builtin_amdgcn_permlane32_swap(m, m, false, false); m = max(t[0], t[1]); }
  const u64 e0 = __ballot(k0 == m), e1 = __ballot(k1 == m);
  slot = e0 ? __builtin_ctzll(e0) : 64 + __builtin_ctzll(e1);
  key_out = __builtin_amdgcn_readfirstlane(m);
  nin = __builtin_popcountll(__ballot(v0)) + __builtin_popcountll(__ballot(v1));
}
typedef short s16x4 __attribute__((ext_vector_type(4)));
DI bf16x8 tr_frag(const LAS unsigned char* p0, const LAS unsigned char* p1) {
  const s16x4 a = __builtin_amdgcn_ds_read_tr16_b64_v4i16((LAS s16x4*)p0), b = __builtin_amdgcn_ds_read_tr16_b64_v4i16((LAS s16x4*)p1);
  return __builtin_shufflevector(a, b, 0, 1, 2, 3, 4, 5, 6, 7);
}
constexpr int p1_unit_bar(int u) { return u == 1 ? -1 : u == 2 ? 0 : u <= 4 ? 1 : u - 3; }
constexpr int p1_unit_ops(int u) { return u < 4 ? 8 : 4; }
constexpr int p1_extra(int b) { return (b == A0W ? 4 : 0) + (b >= 5 ? 4 : 0) + (b == T4U ? 1 : 0); }
constexpr int p1_younger(int u) {
  int n = 0;
  for (int v = u + 1; v < 8; ++v) if (p1_unit_bar(v) < u) n += p1_unit_ops(v);
  for (int b = p1_unit_bar(u); b < u; ++b) n += p1_extra(b);
  return n;
}
template <int N> DI void waitv_c() { asm volatile("s_waitcnt vmcnt(%0)" ::"n"(N) : "memory"); }
DI void p1_wait(int u) {
  switch (u) {
    case 1: waitv_c<p1_younger(1)>(); break; case 2: waitv_c<p1_younger(2)>(); break; case 3: waitv_c<p1_younger(3)>(); break;
    case 4: waitv_c<p1_younger(4)>(); break; case 5: waitv_c<p1_younger(5)>(); break; case 6: waitv_c<p1_younger(6)>(); break;
    case 7: waitv_c<p1_younger(7)>(); break; default: break;
  }
}

constexpr size_t WS_AB = 0;
constexpr size_t WS_PART = WS_AB + (size_t)4096 * 512 * 2;
constexpr size_t WS_BASE = WS_PART + (size_t)256 * 32768;
constexpr size_t WS_FLAG = WS_BASE + (size_t)128 * 4096;
constexpr int FL_HN = 0;
constexpr int FL_Y2 = 128 * 2048;
constexpr int FL_AB = FL_Y2 + 32 * 56 * 64;
constexpr int FL_ARR = FL_AB + 256 * 512;
constexpr int FL_CAND = FL_ARR + 128 * 256;
constexpr int FL_DONE = FL_CAND + 16 * 1024;
constexpr int FL_STAT = FL_DONE + 128 * 128;
constexpr int FL_EPOCH = FL_STAT + 2048 * 16;
constexpr unsigned SPIN_CAP = 1u << 18;

constexpr int LD_D = 0, LD_X = 65536, LD_B = 0, LD_XR = 7 * 16384;
__host__ __device__ constexpr int LD_A(int slot) { return (slot == 0 ? 8 : 3 + slot) * 16384; }
constexpr int SC_ST = 147456;
constexpr int SC_SD = SC_ST + 2048;
constexpr int SC_PF = SC_SD + 256;
constexpr int SC_RED = SC_PF + 1024;
constexpr int SC_X = SC_RED + 2048;
constexpr int SC_GU = SC_X + 2048;
constexpr int SC_ACT = SC_GU + 2048;
constexpr int SC_CTL = SC_ACT + 1024;
constexpr int LDS_BYTES = SC_CTL + 64;
static_assert(LDS_BYTES <= 163840, "LDS");

__global__ __launch_bounds__(512, 2) void fused_kernel(const u16* __restrict__ x, const u16* __restrict__ cr,
                                                       const u16* __restrict__ ci, const u16* __restrict__ dr,
                                                       const u16* __restrict__ di, const u16* __restrict__ wr,
                                                       const u16* __restrict__ wg_, const u16* __restrict__ wu_,
                                                       const u16* __restrict__ wd_, unsigned char* ws, u16* out) {
  extern __shared__ __attribute__((aligned(16))) unsigned char smem[];
  LAS unsigned char* L = (LAS unsigned char*)smem;
  const int tid = threadIdx.x, w8 = __builtin_amdgcn_readfirstlane(tid >> 6), w = w8 & 3, l = tid & 63, r = l & 31, h = l >> 5;
  const int b = blockIdx.x, xcd = b & 7, j = b >> 3;
  const unsigned epoch_v = __hip_atomic_load((GAS unsigned*)(ws + WS_FLAG + FL_EPOCH), RLX_AGENT);

  const int isB = xcd >> 2, sb = ((xcd >> 1) & 1) * 8 + (xcd & 1) * 4 + (j & 3), e8 = j >> 2;
  const u16* dm = (isB ? di : dr) + (size_t)e8 * 64 * D_;
  const auto rx = RSRC(x + (size_t)sb * 128 * D_, 128 * 1024);
  const auto rd = RSRC(dm, 64 * 1024);
  const int sz0 = (((l & 7) ^ ((l >> 4) & 7)) * 16), sz1 = (((l & 7) ^ ((4 + (l >> 4)) & 7)) * 16);
  const int v0 = (16 * w + (l >> 3)) * 1024 + sz0, v1 = (16 * w + (l >> 3)) * 1024 + sz1 + 7 * 1024;
  const int rot1 = (e8 + (j & 3)) & 3;
  auto issue = [&](int u, bool withD) {
    const int ko = ((u + rot1) & 3) * 256;
    if (withD) {
      LAS unsigned char* bd = L + LD_D + (u & 3) * 16384 + w * 2048;
      DMA(rd, bd, v0, ko, 0, 0);
      DMA(rd, bd, v1, ko, 1024, 0);
      DMA(rd, bd + 8192, v0, ko + 128, 0, 0);
      DMA(rd, bd + 8192, v1, ko + 128, 1024, 0);
    }
    LAS unsigned char* bx = L + LD_X + (u & 3) * 16384 + w * 2048;
    const int so = ((u >> 2) ^ (sb & 1)) * 65536 + ko;
    DMA(rx, bx, v0, so, 0, 0);
    DMA(rx, bx, v1, so, 1024, 0);
    DMA(rx, bx + 8192, v0, so + 128, 0, 0);
    DMA(rx, bx + 8192, v1, so + 128, 1024, 0);
  };
  const int srow = (isB * 8 + e8) * 8 + w * 2 + h;
  const int xro = w8 < 4 ? srow * 1024 + r * 32 : 0x7ffffff0;
  u32x4 xr = __builtin_amdgcn_raw_buffer_load_b128(rx, xro, 0, 0), xr2 = __builtin_amdgcn_raw_buffer_load_b128(rx, xro, 16, 0);
  issue(w8 >> 2, true);
  if (tid < 4) ((LAS unsigned*)(L + SC_CTL))[tid] = 0u;
  const int ks = xcd >> 1, mb = (xcd & 1) * 8 + (j >> 2), nb = j & 3;
  const u16* A = ((ks >> 1) ? ci : cr) + (size_t)mb * 128 * S_ + (ks & 1) * 1024;
  const auto ra = RSRC(A, 127 * 4096 + 2048);
  const auto rb = RSRC(ws + WS_AB + (size_t)(nb * 64 + ks * 16) * 16384, 16 * 16384);
  const int va0 = (32 * w + (l >> 3)) * 4096 + sz0, va1 = (32 * w + (l >> 3)) * 4096 + sz1;
  const int vb = w * 4096 + l * 16;
  const int rot2 = (nb + mb) & 3;
  const int pxor = ((xcd ^ mb) & 1) * 4;
  auto ord = [&](int t) { const int i = ((t + rot2) & 3) + ((t & 4) ^ pxor); return 2 * i + ((t >> 3) ^ (i & 1)); };
  auto stageA = [&](int t_, int buf) {
    const int t = ord(t_);
    DMA(ra, L + LD_A(buf) + w * 4096, va0, t * 128, 0, 0);
    DMA(ra, L + LD_A(buf) + w * 4096, va1, t * 128 + 1 * (32768 - 1024), 1024, 0);
    DMA(ra, L + LD_A(buf) + w * 4096, va0, t * 128 + 2 * (32768 - 1024), 2048, 0);
    DMA(ra, L + LD_A(buf) + w * 4096, va1, t * 128 + 3 * (32768 - 1024), 3072, 0);
  };
  auto stageB = [&](int t_, int buf) {
    const int t = ord(t_);
    DMA(rb, L + LD_B + buf * 16384 + w * 4096, vb, t * 16384, 0, AUX_SC1);
    DMA(rb, L + LD_B + buf * 16384 + w * 4096, vb, t * 16384, 1024, AUX_SC1);
    DMA(rb, L + LD_B + buf * 16384 + w * 4096, vb, t * 16384, 2048, AUX_SC1);
    DMA(rb, L + LD_B + buf * 16384 + w * 4096, vb, t * 16384, 3072, AUX_SC1);
  };
  GAS u64* abfl = (GAS u64*)(ws + WS_FLAG + FL_AB) + ((ks * 16 + mb) * 4 + nb) * 64 + (l & 15);
  const int i16 = l & 15, q4 = l >> 4;
  unsigned tag;
  u64 tag64;
  auto make_tag = [&]() {
    const unsigned e = __builtin_amdgcn_readfirstlane(epoch_v);
    tag = (e + 1u) * 0x9E3779B1u ^ 0x5BD1E995u;
    tag64 = ((u64)tag << 32) | (u64)(e * 0x85EBCA77u + 0x27D4EB2Fu);
  };
  auto half_sum = [&](float v) {
    v += dpp<XOR1>(v); v += dpp<XOR2>(v); v += dpp<XOR7>(v); v += dpp<XOR8>(v);
    return fold16(v, v);
  };
  auto row_stats = [&]() {
    float xv[16];
#pragma unroll
    for (int i = 0; i < 4; ++i) { xv[2 * i] = bf_lo(xr[i]); xv[2 * i + 1] = bf_hi(xr[i]); xv[8 + 2 * i] = bf_lo(xr2[i]); xv[9 + 2 * i] = bf_hi(xr2[i]); }
    float s = 0.f;
#pragma unroll
    for (int i = 0; i < 4; ++i) s += (xv[4 * i] + xv[4 * i + 1]) + (xv[4 * i + 2] + xv[4 * i + 3]);
    const float mean = half_sum(s) * (1.0f / D_);
    float q0 = 0.f, q1 = 0.f;
#pragma unroll
    for (int i = 0; i < 8; ++i) { const float d = xv[i] - mean, e = xv[8 + i] - mean; q0 = fmaf(d, d, q0); q1 = fmaf(e, e, q1); }
    const float rs = __builtin_amdgcn_rsqf(half_sum(q0 + q1) * (1.0f / D_) + 1e-5f);
    if (r < 2) __hip_atomic_store((GAS u64*)(ws + WS_FLAG + FL_STAT) + (sb * 128 + srow) * 2 + r, ((u64)tag << 32) | (u64)__float_as_uint(r ? rs : mean * rs), RLX_AGENT);
  };

  if (w8 >= 4) {
    f32x4 sdv = {};
    const bf16x8 ones = {0x3f80, 0x3f80, 0x3f80, 0x3f80, 0x3f80, 0x3f80, 0x3f80, 0x3f80};
    const int sdd = LD_D + (w * 16 + i16) * 128;
    const int ps0 = (q4 ^ (i16 >> 1)) * 16, ps1 = ((4 + q4) ^ (i16 >> 1)) * 16;
#pragma unroll
    for (int u = 0; u < 8; ++u) {
      p1_wait(u);
      WAITL();
      BAR();
      bf16x8 d[4];
      if (u < 4) {
        const LAS unsigned char* bb = L + sdd + u * 16384;
        d[0] = *(const LAS bf16x8*)(bb + ps0); d[1] = *(const LAS bf16x8*)(bb + ps1);
        d[2] = *(const LAS bf16x8*)(bb + 8192 + ps0); d[3] = *(const LAS bf16x8*)(bb + 8192 + ps1);
      }
      SB();
      if (u == 0) issue(2, true);
      if (u == 1) { issue(3, true); issue(4, false); }
      if (u >= 2 && u <= 4) issue(u + 3, false);
      if (u == A0W) stageA(0, 0);
      if (u >= 5) stageA(u - 4, u - 4);
      if (u == T4U)
        __builtin_amdgcn_raw_ptr_buffer_load_lds(ra, (LAS void*)(L + SC_PF + w * 256), 4, h ? 0x7ffffff0 : (32 * w + r) * 4096, ord(4) * 128, 0, 0);
      if (u == 1) make_tag();
      SB();
      if (u < 4) {
#pragma unroll
        for (int i = 0; i < 4; ++i) sdv = MFMA16(d[i], ones, sdv);
      }
      if (u == 3) { if (i16 == 0) *(LAS f32x4*)(L + SC_SD + (w * 16 + q4 * 4) * 4) = sdv; }
      SB();
    }
    if (w8 == 4) {
      for (unsigned spins = 0;; ++spins) {
        if (__all(__hip_atomic_load(abfl, RLX_AGENT) == tag64) || spins > SPIN_CAP) break;
        __builtin_amdgcn_s_sleep(1);
      }
    }
    WAITV(0);
    BAR();
    stageB(0, 0); stageB(1, 1);
    WAITV(4);
    BAR();
    stageB(2, 2); stageB(3, 3);
    WAITV(8); BAR(); stageA(4, 0); stageB(4, 0);
    WAITV(12); BAR(); stageA(5, 1); stageB(5, 1);
#pragma unroll
    for (int t = 2; t < 12; ++t) {
      WAITV(16);
      BAR();
      stageA(t + 4, t & 3); stageB(t + 4, t & 3);
    }
    {
      const bool rowrole = (j & 1) == 0;
      const auto rxr = RSRC(x + (size_t)((mb * 8 + ks * 2 + (nb >> 1)) * 16) * D_, rowrole ? 16 * 1024 : 0);
      const auto rwr = RSRC(wr, rowrole ? 1024 : 0);
#pragma unroll
      for (int i = 0; i < 4; ++i) DMA(rxr, L + LD_XR + (4 * w + i) * 1024, (l ^ (4 * w + i)) * 16, (4 * w + i) * 1024, 0, 0);
      DMA(rwr, L + SC_PF, l * 16, 0, 0, 0);
    }
    WAITV(21); BAR(); WAITV(13); BAR(); WAITV(5); BAR();
    WAITV(0);
    if (j & 1) {
      BAR();
      const int o2 = j >> 1;
      if (o2 < 7) {
        const int fp = xcd * 7 + o2;
        const auto rg = RSRC(wg_, D_ * F_ * 2);
        const auto ru = RSRC(wu_, D_ * F_ * 2);
        const auto rw = RSRC(wd_ + (size_t)fp * 32 * D_, 32 * D_ * 2);
        const int vg = (w * 16 + (l >> 2)) * (F_ * 2) + fp * 64;
        const int cg = (l & 3) ^ (2 * (l >> 5));
#pragma unroll
        for (int i = 0; i < 8; ++i) {
          DMA(rg, L + (i * 4 + w) * 1024, vg + cg * 16, i * 64 * F_ * 2, 0, 0);
          DMA(ru, L + 32768 + (i * 4 + w) * 1024, vg + (cg ^ 1) * 16, i * 64 * F_ * 2, 0, 0);
        }
#pragma unroll
        for (int i = 0; i < 8; ++i) DMA(rw, L + 65536 + (w * 8 + i) * 1024, (l ^ (2 * (i & 3) + 8 * (w & 1))) * 16, (w * 8 + i) * 1024, 0, 0);
      }
    }
  } else {
    const int nbp = e8 >> 1;
    auto publish = [&](int half) {
      WAITV(0);
      unsigned arr1 = 0;
      if (l == 0) arr1 = __hip_atomic_fetch_add((LAS unsigned*)(L + SC_CTL) + half * 3, 1u, __ATOMIC_RELAXED, __HIP_MEMORY_SCOPE_WORKGROUP);
      if (__builtin_amdgcn_readfirstlane(arr1) == 3u && l < 16)
        __hip_atomic_store((GAS u64*)(ws + WS_FLAG + FL_AB) + (((isB * 2 + (sb >> 3)) * 16 + l) * 4 + nbp) * 64 + half * 16 + (sb & 7) * 2 + (e8 & 1), tag64, RLX_AGENT);
    };
    {
      const int rg = w & 1, nt1 = w >> 1;
      f32x16 acc[2] = {};
      bf16x8 fx[2][8], fd[2][8];
      int pos[4];
#pragma unroll
      for (int kk = 0; kk < 4; ++kk) pos[kk] = r * 128 + (((2 * kk + h) ^ ((r >> 1) & 7)) * 16);
      auto ldf = [&](int u, int k8) {
        fx[u & 1][k8] = *(const LAS bf16x8*)(L + LD_X + (u & 3) * 16384 + rg * 4096 + (k8 >> 2) * 8192 + pos[k8 & 3]);
        fd[u & 1][k8] = *(const LAS bf16x8*)(L + LD_D + (u & 3) * 16384 + nt1 * 4096 + (k8 >> 2) * 8192 + pos[k8 & 3]);
      };
      GAS u64* stp = (GAS u64*)(ws + WS_FLAG + FL_STAT) + (sb * 128 + rg * 32) * 2 + l;
      const int hsw = sb & 1;
      u64 sg[2] = {0, 0};
      LAS float* st = (LAS float*)(L + SC_ST + w * 512);
      auto epilogue = [&](int half) {
        for (unsigned spins = 0; !__all((unsigned)(sg[half] >> 32) == tag) && spins <= SPIN_CAP; ++spins) sg[half] = __hip_atomic_load(stp + (half ^ hsw) * 128, RLX_AGENT);
        st[half * 64 + l] = __uint_as_float((unsigned)sg[half] ^ (isB ? 0x80000000u : 0u));
        const float cs = ((const LAS float*)(L + SC_SD))[nt1 * 32 + r];
        const LAS f32x4* st4 = (const LAS f32x4*)(st + half * 64);
        const f32x16& a = acc[half];
        unsigned p0[8];
#pragma unroll
        for (int g = 0; g < 4; ++g) {
          const f32x4 sa = st4[4 * g + 2 * h], sbv = st4[4 * g + 2 * h + 1];
          p0[2 * g] = pk_bf16(a[4 * g] * sa[1] - sa[0] * cs, a[4 * g + 1] * sa[3] - sa[2] * cs);
          p0[2 * g + 1] = pk_bf16(a[4 * g + 2] * sbv[1] - sbv[0] * cs, a[4 * g + 3] * sbv[3] - sbv[2] * cs);
        }
        const auto rab = RSRC(ws + WS_AB + (size_t)(nbp * 64 + isB * 32 + sb * 2 + (half ^ hsw)) * 16384, 16384);
#pragma unroll
        for (int s2 = 0; s2 < 2; ++s2) {
          auto t0 = __builtin_amdgcn_permlane32_swap(p0[4 * s2], p0[4 * s2 + 2], false, false);
          auto t1 = __builtin_amdgcn_permlane32_swap(p0[4 * s2 + 1], p0[4 * s2 + 3], false, false);
          u32x4 o = {t0[0], t1[0], t0[1], t1[1]};
          __builtin_amdgcn_raw_buffer_store_b128(o, rab, l * 16, ((rg * 2 + s2) * 4 + (e8 & 1) * 2 + nt1) * 1024, AUX_SC1);
        }
      };
      make_tag();
      row_stats();
      WAITV(1);
      BAR();
#pragma unroll
      for (int k8 = 0; k8 < 8; ++k8) ldf(0, k8);
#pragma unroll
      for (int u = 0; u < 8; ++u) {
        const int p = u & 1;
        if (u < 7) { WAITL(); BAR(); }
        if (u == 2) { sg[0] = __hip_atomic_load(stp + hsw * 128, RLX_AGENT); sg[1] = __hip_atomic_load(stp + (hsw ^ 1) * 128, RLX_AGENT); SB(); }
#pragma unroll
        for (int k8 = 0; k8 < 8; ++k8) {
          if (u < 7) ldf(u + 1, k8);
          SB();
          acc[u >> 2] = MFMA32(fx[p][k8], fd[p][k8], acc[u >> 2]);
          SB();
        }
        if (u == 3) { epilogue(0); SB(); publish(0); SB(); }
      }
      epilogue(1);
    }
    const int wm = w >> 1, wn = w & 1;
    f32x16 acc[2][2] = {};
    int aoff[4];
    const int sw = (r >> 1) & 7;
#pragma unroll
    for (int kk = 0; kk < 4; ++kk) aoff[kk] = (wm * 64 + r) * 128 + (((2 * kk + h) ^ sw) * 16);
    const int boff = LD_B + wn * 2048 + l * 16;
    bf16x8 fa[2][2], fb[2][2];
    auto ldf = [&](int t, int kk, int p) {
      const int bo = (t & 3) * 16384;
      fb[p][0] = *(const LAS bf16x8*)(L + bo + boff + kk * 4096);
      fa[p][0] = *(const LAS bf16x8*)(L + LD_A(t & 3) + aoff[kk]);
      fa[p][1] = *(const LAS bf16x8*)(L + LD_A(t & 3) + aoff[kk] + 4096);
      fb[p][1] = *(const LAS bf16x8*)(L + bo + boff + kk * 4096 + 1024);
    };
    auto mma = [&](int p) {
      acc[0][0] = MFMA32(fb[p][0], fa[p][0], acc[0][0]);
      acc[1][0] = MFMA32(fb[p][0], fa[p][1], acc[1][0]);
      acc[0][1] = MFMA32(fb[p][1], fa[p][0], acc[0][1]);
      acc[1][1] = MFMA32(fb[p][1], fa[p][1], acc[1][1]);
      SB();
    };
    WAITL();
    BAR();
    publish(1);
    BAR();
    u64 fl2 = 0;
    ldf(0, 0, 0);
#pragma unroll
    for (int t = 0; t < 16; ++t) {
      if (t + TL == 8 || t + TL == 12) {
        __builtin_amdgcn_raw_ptr_buffer_load_lds(ra, (LAS void*)(L + SC_ST + w * 512), 4, h ? 0x7ffffff0 : (32 * w + r) * 4096, ord(t + TL) * 128, 0, 0);
        SB();
      }
      if (t == 3) { fl2 = __hip_atomic_load(abfl + 16, RLX_AGENT); SB(); }
      ldf(t, 1, 1); SB(); mma(0);
      ldf(t, 2, 0); SB(); mma(1);
      ldf(t, 3, 1); SB(); mma(0);
      if (t == 4 && w == 0) {
        for (unsigned spins = 0; !__all(fl2 == tag64) && spins <= SPIN_CAP; ++spins) fl2 = __hip_atomic_load(abfl + 16, RLX_AGENT);
      }
      if (t < 15) { WAITL(); BAR(); ldf(t + 1, 0, 0); SB(); }
      mma(1);
    }
    const auto rp = RSRC(ws + WS_PART + (size_t)((ks * 16 + mb) * 4 + nb) * 32768, 32768);
#pragma unroll
    for (int nt = 0; nt < 2; ++nt)
#pragma unroll
      for (int mt = 0; mt < 2; ++mt)
#pragma unroll
        for (int s2 = 0; s2 < 2; ++s2) {
          const f32x16& a = acc[mt][nt];
          const unsigned p0 = pk_bf16(a[8 * s2], a[8 * s2 + 1]), p1 = pk_bf16(a[8 * s2 + 2], a[8 * s2 + 3]);
          const unsigned p2 = pk_bf16(a[8 * s2 + 4], a[8 * s2 + 5]), p3 = pk_bf16(a[8 * s2 + 6], a[8 * s2 + 7]);
          auto t0 = __builtin_amdgcn_permlane32_swap(p0, p2, false, false);
          auto t1 = __builtin_amdgcn_permlane32_swap(p1, p3, false, false);
          const u32x4 o = {t0[0], t1[0], t0[1], t1[1]};
          __builtin_amdgcn_raw_buffer_store_b128(o, rp, (((w * 2 + mt) * 2 + nt) * 2 + s2) * 1024 + l * 16, 0, AUX_SC1);
          SB();
        }
    WAITV(0);
    unsigned arr2 = 0;
    if (l == 0) arr2 = __hip_atomic_fetch_add((LAS unsigned*)(L + SC_CTL) + 1 + wm, 1u, __ATOMIC_RELAXED, __HIP_MEMORY_SCOPE_WORKGROUP);
    if (__builtin_amdgcn_readfirstlane(arr2) == 1u && l < 4)
      __hip_atomic_store((GAS u64*)(ws + WS_FLAG + FL_ARR) + (mb * 8 + wm * 4 + l) * 32 + ks * 4 + nb, tag64, RLX_AGENT);
    if (j & 1) BAR();
  }
  GAS u64* cand = (GAS u64*)(ws + WS_FLAG + FL_CAND);
  const auto ro = RSRC(out, S_ * D_ * 2);
  const u64 thi = (u64)tag << 32;

  if ((j & 1) == 0) {
    const int q2 = ks * 2 + (nb >> 1), ci_ = mb * 8 + q2;
    const int r16 = l & 15, c = tid >> 4;
    const int row = ci_ * 16 + r16;
    BAR();
    u32x4 xv[2], wv[2];
#pragma unroll
    for (int u = 0; u < 2; ++u) {
      xv[u] = *(const LAS u32x4*)(L + LD_XR + r16 * 1024 + (((c + 32 * u) ^ r16) * 16));
      wv[u] = *(const LAS u32x4*)(L + SC_PF + (c + 32 * u) * 16);
    }
    if (w8 == 5) {
      GAS u64* arr = (GAS u64*)(ws + WS_FLAG + FL_ARR) + ci_ * 32 + (l & 15);
      for (unsigned spins = 0;; ++spins) {
        if (__all(__hip_atomic_load(arr, RLX_AGENT) == tag64) || spins > SPIN_CAP) break;
        __builtin_amdgcn_s_sleep(1);
      }
    }
    const auto rp = RSRC(ws + WS_PART, 256 * 32768);
    const int poff = (mb * 4 + (c >> 4)) * 32768 +
                     (((((q2 >> 2) * 2 + ((c >> 3) & 1)) * 2 + ((q2 >> 1) & 1)) * 2 + ((c >> 2) & 1)) * 2 + ((c >> 1) & 1)) * 1024 +
                     ((c & 1) * 32 + (q2 & 1) * 16 + r16) * 16;
    SB();
    BAR();
    u32x4 pv[2][4];
#pragma unroll
    for (int u = 0; u < 2; ++u)
#pragma unroll
      for (int k2 = 0; k2 < 4; ++k2) pv[u][k2] = __builtin_amdgcn_raw_buffer_load_b128(rp, poff, (k2 * 64 + u * 2) * 32768, AUX_SC1);
    float sw = 0.f, wf[16], hv[16];
#pragma unroll
    for (int u = 0; u < 2; ++u)
#pragma unroll
      for (int i = 0; i < 4; ++i) {
        wf[8 * u + 2 * i] = bf_lo(wv[u][i]); wf[8 * u + 2 * i + 1] = bf_hi(wv[u][i]);
        sw += wf[8 * u + 2 * i] + wf[8 * u + 2 * i + 1];
        hv[8 * u + 2 * i] = bf_lo(xv[u][i]) + ((bf_lo(pv[u][0][i]) + bf_lo(pv[u][1][i])) + (bf_lo(pv[u][2][i]) + bf_lo(pv[u][3][i])));
        hv[8 * u + 2 * i + 1] = bf_hi(xv[u][i]) + ((bf_hi(pv[u][0][i]) + bf_hi(pv[u][1][i])) + (bf_hi(pv[u][2][i]) + bf_hi(pv[u][3][i])));
      }
    float mu = 0.f, m2 = 0.f, hw = 0.f;
#pragma unroll
    for (int i = 0; i < 4; ++i) mu += (hv[4 * i] + hv[4 * i + 1]) + (hv[4 * i + 2] + hv[4 * i + 3]);
    mu *= 0.0625f;
#pragma unroll
    for (int i = 0; i < 16; ++i) { const float d = hv[i] - mu; m2 = fmaf(d, d, m2); hw = fmaf(hv[i], wf[i], hw); }
    float hs = fold16(hw, sw);
    {
      auto t = __builtin_amdgcn_permlane16_swap(__float_as_uint(mu), __float_as_uint(mu), false, false);
      const float a = __uint_as_float(t[0]), bb = __uint_as_float(t[1]), d = a - bb;
      m2 = fold16(m2, m2) + d * d * 8.0f; mu = (a + bb) * 0.5f;
    }
    hs = fold32(hs, hs);
    {
      auto t = __builtin_amdgcn_permlane32_swap(__float_as_uint(mu), __float_as_uint(mu), false, false);
      const float a = __uint_as_float(t[0]), bb = __uint_as_float(t[1]), d = a - bb;
      m2 = fold32(m2, m2) + d * d * 16.0f; mu = (a + bb) * 0.5f;
    }
    LAS float* red = (LAS float*)(L + SC_RED);
    if (l < 16) *(LAS f32x2*)(red + (w8 * 16 + l) * 4) = (f32x2){mu, m2};
    if (l < 32) red[(w8 * 16 + r16) * 4 + 2 + (l >> 4)] = hs;
    WAITL();
    BAR();
    float mean, M2, HW, SW;
    {
      const f32x4 ta = *(const LAS f32x4*)(red + l * 4), tb = *(const LAS f32x4*)(red + (64 + l) * 4);
      float ms = ta[0] + tb[0];
      float hs2 = fold16(ta[2] + tb[2], ta[3] + tb[3]);
      ms = fold16(ms, ms); ms = fold32(ms, ms);
      mean = ms * 0.125f;
      const float da = ta[0] - mean, db = tb[0] - mean;
      float q = (ta[1] + tb[1]) + 64.0f * (da * da + db * db);
      hs2 = fold32(hs2, hs2);
      q = fold16(q, q); M2 = fold32(q, q);
      auto t = __builtin_amdgcn_permlane16_swap(__float_as_uint(hs2), __float_as_uint(hs2), false, false);
      HW = __uint_as_float(t[0]); SW = __uint_as_float(t[1]);
    }
    const float rstd = __builtin_amdgcn_rsqf(M2 * (1.0f / D_) + 1e-5f);
    const float rr = (HW - mean * SW) * rstd;
    unsigned key = (ord_f32(rr) & ~15u) | (unsigned)(15 - r16);
    key = max(key, dppu<XOR1>(key)); key = max(key, dppu<XOR2>(key));
    key = max(key, dppu<XOR7>(key)); key = max(key, dppu<XOR8>(key));
    if (tid < 16) __hip_atomic_store(cand + tid * 128 + ci_, thi | (u64)key, RLX_AGENT);
    const bool best = (15 - (int)(key & 15u)) == r16;
    float hn[16], ob[16];
#pragma unroll
    for (int i = 0; i < 16; ++i) { hn[i] = (hv[i] - mean) * rstd; ob[i] = hv[i] + hn[i]; }
    LAS unsigned* rowh = (LAS unsigned*)(L + SC_GU);
    LAS float* rowb = (LAS float*)(L + SC_X);
    if (best) {
#pragma unroll
      for (int u = 0; u < 2; ++u) {
        *(LAS u32x4*)(rowh + (c + 32 * u) * 4) = (u32x4){pk_bf16(hn[8 * u], hn[8 * u + 1]), pk_bf16(hn[8 * u + 2], hn[8 * u + 3]),
                                                         pk_bf16(hn[8 * u + 4], hn[8 * u + 5]), pk_bf16(hn[8 * u + 6], hn[8 * u + 7])};
        *(LAS f32x4*)(rowb + (c + 32 * u) * 8) = (f32x4){ob[8 * u], ob[8 * u + 1], ob[8 * u + 2], ob[8 * u + 3]};
        *(LAS f32x4*)(rowb + (c + 32 * u) * 8 + 4) = (f32x4){ob[8 * u + 4], ob[8 * u + 5], ob[8 * u + 6], ob[8 * u + 7]};
      }
    }
    WAITL();
    BAR();
    if (w8 < 4) __hip_atomic_store((GAS u64*)(ws + WS_FLAG + FL_HN + ci_ * 2048) + tid, thi | (u64)rowh[tid], RLX_AGENT);
    if (w8 == 7) {
      const f32x4 a = *(const LAS f32x4*)(rowb + l * 8), bq = *(const LAS f32x4*)(rowb + l * 8 + 4);
      const u32x4 o = {pk_bf16(a[0], a[1]), pk_bf16(a[2], a[3]), pk_bf16(bq[0], bq[1]), pk_bf16(bq[2], bq[3])};
      __builtin_amdgcn_raw_buffer_store_b128(o, ro, ((ci_ * 16 + 15 - (int)(key & 15u)) * D_ + l * 8) * 2, 0, AUX_SC1);
    }
    __hip_atomic_store((GAS u64*)(ws + WS_BASE + ci_ * 4096) + tid, thi | (u64)__float_as_uint(rowb[tid]), RLX_AGENT);
    if (w8 == 7) {
      WAITV(0);
      if (l == 0) __hip_atomic_store((GAS u64*)(ws + WS_FLAG + FL_DONE) + ci_ * 16, tag64, RLX_AGENT);
    }
    SB();
    if (!best) {
#pragma unroll
      for (int u = 0; u < 2; ++u) {
        const u32x4 o = {pk_bf16(ob[8 * u], ob[8 * u + 1]), pk_bf16(ob[8 * u + 2], ob[8 * u + 3]),
                         pk_bf16(ob[8 * u + 4], ob[8 * u + 5]), pk_bf16(ob[8 * u + 6], ob[8 * u + 7])};
        __builtin_amdgcn_raw_buffer_store_b128(o, ro, (row * D_ + (c + 32 * u) * 8) * 2, 0, AUX_SC1);
      }
    }
    return;
  }
  const int o2 = j >> 1;
  if (o2 >= 7) {
    const int slot = (o2 - 7) * 8 + w8;
    const bool gu = slot >= 28;
    const int g = 91 - slot;
    const u16* wp = gu ? ((g & 1) ? wu_ : wg_) : wd_ + (size_t)xcd * 224 * D_;
    const auto rq = RSRC(wp, gu ? D_ * F_ * 2 : 224 * D_ * 2);
    const int vo = gu ? ((g >> 1) * 16 + (l >> 2)) * (F_ * 2) + ((xcd * 448) & ~127) + (l & 3) * 128 : (slot * 64 + l) * 128;
    __builtin_amdgcn_raw_ptr_buffer_load_lds(rq, (LAS void*)(L + w8 * 256), 4, vo, 0, 0, 0);
  }
  if (o2 < 7) {
    const int fp = xcd * 7 + o2;
    bf16x8 gf[16], df[4];
    WAITV(0);
    BAR();
    {
      const int q = (l & 15) >> 2, p = l & 3;
      if (w8 < 4) {
        const LAS unsigned char* pg = L + (p >> 1) * 32768 + (8 * q4 + q) * 64 + ((w ^ (2 * (q4 & 1)) ^ (p >> 1)) * 16) + (p & 1) * 8;
#pragma unroll
        for (int k2 = 0; k2 < 16; ++k2) gf[k2] = tr_frag(pg + k2 * 2048, pg + k2 * 2048 + 256);
      } else {
#pragma unroll
        for (int k2 = 0; k2 < 16; ++k2) gf[k2] = (bf16x8){};
      }
      const LAS unsigned char* pd = L + 65536 + (8 * q4 + q) * 1024 + (p & 1) * 8;
#pragma unroll
      for (int t = 0; t < 4; ++t) {
        const int ch = ((w8 * 8 + t * 2 + (p >> 1)) ^ (2 * q + 8 * (q4 & 1))) * 16;
        df[t] = tr_frag(pd + ch, pd + ch + 4096);
      }
#pragma unroll
      for (int k2 = 0; k2 < 16; ++k2) asm volatile("" : "+v"(gf[k2]));
#pragma unroll
      for (int t = 0; t < 4; ++t) asm volatile("" : "+v"(df[t]));
    }
    if (w8 == 4) {
      GAS u64* cp = cand + xcd * 128 + l;
      int lead = 0;
      for (unsigned spins = 0;; ++spins) {
        const u64 c0 = __hip_atomic_load(cp, RLX_AGENT), c1 = __hip_atomic_load(cp + 64, RLX_AGENT);
        unsigned key; int nin;
        best_candidate(c0, c1, tag, key, lead, nin);
        if (nin >= 64 || spins > SPIN_CAP) break;
        __builtin_amdgcn_s_sleep(8);
      }
      for (unsigned spins = 0;; ++spins) {
        const u64 c0 = __hip_atomic_load(cp, RLX_AGENT), c1 = __hip_atomic_load(cp + 64, RLX_AGENT);
        GAS u64* hp = (GAS u64*)(ws + WS_FLAG + FL_HN + lead * 2048) + l * 4;
        const u64 g0 = __hip_atomic_load(hp, RLX_AGENT), g1 = __hip_atomic_load(hp + 1, RLX_AGENT);
        const u64 g2 = __hip_atomic_load(hp + 2, RLX_AGENT), g3 = __hip_atomic_load(hp + 3, RLX_AGENT);
        unsigned key; int wc, nin;
        best_candidate(c0, c1, tag, key, wc, nin);
        *(LAS u32x4*)(L + SC_X + l * 16) = (u32x4){(unsigned)g0, (unsigned)g1, (unsigned)g2, (unsigned)g3};
        const bool ok = ((unsigned)(g0 >> 32) == tag) & ((unsigned)(g1 >> 32) == tag) & ((unsigned)(g2 >> 32) == tag) & ((unsigned)(g3 >> 32) == tag);
        if ((nin == 128 && wc == lead && __all(ok)) || spins > SPIN_CAP) break;
        lead = wc;
        if (nin < 128) __builtin_amdgcn_s_sleep(1);
      }
    }
    WAITL();
    BAR();
    if (w8 < 4) {
      f32x4 gu = {};
#pragma unroll
      for (int k2 = 0; k2 < 16; ++k2) gu = MFMA16(*(const LAS bf16x8*)(L + SC_X + k2 * 64 + q4 * 16), gf[k2], gu);
      const float g = gu[0], u = dpp<XOR8>(g);
      const float act = g * __builtin_amdgcn_rcpf(1.0f + __expf(-g)) * u;
      const unsigned pr = pk_bf16(act, dpp<XOR1>(act));
      if ((l & 0x39) == 0) ((LAS unsigned*)(L + SC_ACT))[w * 4 + (l >> 1)] = pr;
    }
    WAITL();
    BAR();
    {
      const bf16x8 af = *(const LAS bf16x8*)(L + SC_ACT + q4 * 16);
      const f32x4 z = {};
      const f32x4 y0 = MFMA16(af, df[0], z), y1 = MFMA16(af, df[1], z), y2 = MFMA16(af, df[2], z), y3 = MFMA16(af, df[3], z);
      const unsigned m0 = 0u - (unsigned)(q4 & 1), m1 = 0u - (unsigned)(q4 >> 1);
      const float y = bsel(m1, bsel(m0, y3[0], y2[0]), bsel(m0, y1[0], y0[0]));
      const float yn = dpp<XOR1>(y);
      if ((l & 1) == 0) {
        const int cp = tid >> 1;
        __hip_atomic_store((GAS u64*)(ws + WS_FLAG + FL_Y2 + ((cp >> 3) * 56 + fp) * 64) + (cp & 7), thi | (u64)pk_bf16(y, yn), RLX_AGENT);
      }
    }
  } else if (o2 < 11 && w8 == 4) {
    const int cb = xcd * 4 + (o2 - 7);
    unsigned whi;
    u64 b0, b1;
    float y0, y1;
    int lead = 0;
    GAS u64* cp = cand + (8 + xcd) * 128 + l;
    __builtin_amdgcn_s_sleep(64);
    for (unsigned spins = 0;; ++spins) {
      const u64 c0 = __hip_atomic_load(cp, RLX_AGENT), c1 = __hip_atomic_load(cp + 64, RLX_AGENT);
      int nin;
      best_candidate(c0, c1, tag, whi, lead, nin);
      if (nin == 128 || spins > SPIN_CAP) break;
      __builtin_amdgcn_s_sleep(4);
    }
    const int wrow = lead * 16 + 15 - (int)(whi & 15u);
    const float sig = 1.0f / (1.0f + __expf(-unord_f32(whi)));
    for (unsigned spins = 0;; ++spins) {
      u64 g[7];
#pragma unroll
      for (int i = 0; i < 7; ++i) g[i] = __hip_atomic_load((GAS u64*)(ws + WS_FLAG + FL_Y2 + cb * 56 * 64) + l + i * 64, RLX_AGENT);
      const u64 dn = __hip_atomic_load((GAS u64*)(ws + WS_FLAG + FL_DONE) + lead * 16, RLX_AGENT);
      GAS u64* bp = (GAS u64*)(ws + WS_BASE + lead * 4096) + cb * 16 + (l & 7) * 2;
      b0 = __hip_atomic_load(bp, RLX_AGENT); b1 = __hip_atomic_load(bp + 1, RLX_AGENT);
      bool ok = (dn == tag64) & ((unsigned)(b0 >> 32) == tag) & ((unsigned)(b1 >> 32) == tag);
      y0 = 0.f; y1 = 0.f;
#pragma unroll
      for (int i = 0; i < 7; ++i) { ok &= (unsigned)(g[i] >> 32) == tag; y0 += bf_lo((unsigned)g[i]); y1 += bf_hi((unsigned)g[i]); }
      if (__all(ok) || spins > SPIN_CAP) break;
    }
    y0 += dpp<XOR8>(y0); y1 += dpp<XOR8>(y1);
    float yy = fold16(y0, y1);
    yy = fold32(yy, yy);
    auto t = __builtin_amdgcn_permlane16_swap(__float_as_uint(yy), __float_as_uint(yy), false, false);
    const float f0 = __uint_as_float(t[0]), f1 = __uint_as_float(t[1]);
    if (l < 8)
      __builtin_amdgcn_raw_buffer_store_b32(pk_bf16(__uint_as_float((unsigned)b0) + f0 * sig, __uint_as_float((unsigned)b1) + f1 * sig),
                                            ro, (wrow * D_ + cb * 16 + l * 2) * 2, 0, AUX_SC1);
    if (cb == 0 && l == 0) __hip_atomic_store((GAS unsigned*)(ws + WS_FLAG + FL_EPOCH), epoch_v + 1u, RLX_AGENT);
  }
}

extern "C" void kernel_launch(void* const* d_in, const int* in_sizes, int n_in, void* d_out, int out_size, void* d_ws,
                              size_t ws_size, hipStream_t stream) {
  const u16* x = (const u16*)d_in[0];
  const u16* cr = (const u16*)d_in[1];
  const u16* ci = (const u16*)d_in[2];
  const u16* dr = (const u16*)d_in[3];
  const u16* di = (const u16*)d_in[4];
  const u16* wr = (const u16*)d_in[6];
  const u16* wg = (const u16*)d_in[7];
  const u16* wu = (const u16*)d_in[8];
  const u16* wd = (const u16*)d_in[9];
  (void)hipFuncSetAttribute((const void*)fused_kernel, hipFuncAttributeMaxDynamicSharedMemorySize, LDS_BYTES);
  fused_kernel<<<256, 512, LDS_BYTES, stream>>>(x, cr, ci, dr, di, wr, wg, wu, wd, (unsigned char*)d_ws, (u16*)d_out);
}
```
